```python
import torch

def _softmax(x, axis=-1):
    return torch.softmax(x, dim=axis)
def _gelu(x):
    return torch.nn.functional.gelu(x, approximate="tanh")
def _silu(x):
    return torch.nn.functional.silu(x)
def _sigmoid(x):
    return torch.sigmoid(x)
def _layernorm(x, axis=-1, eps=1e-5):
    m = x.mean(dim=axis, keepdim=True)
    v = x.var(dim=axis, keepdim=True, unbiased=False)
    return (x - m) / torch.sqrt(v + eps)
def _scatter_add(tgt, idx, src, axis=0):
    return tgt.clone().index_add_(axis, idx, src)
def _segment_sum(src, seg, n):
    out = torch.zeros((n,) + src.shape[1:], dtype=src.dtype)
    return out.index_add_(0, seg, src)
def _pad(x, pw):
    flat = tuple(v for pair in reversed(pw) for v in pair)
    return torch.nn.functional.pad(x, flat)

kernel_name = 'parallel_sum__sliding_block__swiglu_ffn__prompt'

def setup_inputs():

    _w = (lambda a: (lambda t: t.to(torch.bfloat16) if t.is_floating_point() else t)(torch.as_tensor(a)))
    def _A_setup():
        B, S, D = 1, 2048, 640
        return {
            "x": _w((lambda *s: torch.randn(*s, dtype=torch.bfloat16) * 0.05)(B, S, D)),
            "wqkv": _w((lambda *s: torch.randn(*s, dtype=torch.bfloat16) * 0.05)(D, 3 * D)),
            "wo": _w((lambda *s: torch.randn(*s, dtype=torch.bfloat16) * 0.05)(D, D)),
        }
    _A_in = _A_setup()

    def _B_setup():
        B, S, D, F = 1, 2048, 640, 1536
        return {
            "x": _w((lambda *s: torch.randn(*s, dtype=torch.bfloat16) * 0.05)(B, S, D)),
            "w_gate": _w((lambda *s: torch.randn(*s, dtype=torch.bfloat16) * 0.05)(D, F)),
            "w_up": _w((lambda *s: torch.randn(*s, dtype=torch.bfloat16) * 0.05)(D, F)),
            "w_down": _w((lambda *s: torch.randn(*s, dtype=torch.bfloat16) * 0.05)(F, D)),
        }
    _B_in = _B_setup()

    return {
        "A_x": _A_in["x"],
        "A_wqkv": _A_in["wqkv"],
        "A_wo": _A_in["wo"],
        "B_x": _B_in["x"],
        "B_w_gate": _B_in["w_gate"],
        "B_w_up": _B_in["w_up"],
        "B_w_down": _B_in["w_down"],
    }

def reference(A_x, A_wqkv, A_wo, B_x, B_w_gate, B_w_up, B_w_down):
    def _A_ref(x, wqkv, wo):
        B, S, D = x.shape
        H = 32; Dh = D // H; W = 256
        qkv = (lambda x, *d: x.permute(*d))((x @ wqkv).reshape(B, S, 3, H, Dh), 2, 0, 3, 1, 4)
        q, k, v = qkv[0], qkv[1], qkv[2]
        scores = torch.einsum("bhqd,bhkd->bhqk", q, k) * (Dh ** -0.5)
        idx = torch.arange(S)
        mask = (idx[:, None] - idx[None, :] >= 0) & (idx[:, None] - idx[None, :] < W)
        scores = torch.where(mask[None, None], scores, -1e9)
        p = _softmax(scores, axis=-1)
        o = torch.einsum("bhqk,bhkd->bhqd", p, v)
        return (lambda x, *d: x.permute(*d))(o, 0, 2, 1, 3).reshape(B, S, D) @ wo

    def _B_ref(x, w_gate, w_up, w_down):
        g = _silu(x @ w_gate)
        u = x @ w_up
        return (g * u) @ w_down

    x = A_x
    xn = _layernorm(x, axis=-1)
    return x + _A_ref(xn, A_wqkv, A_wo) + _B_ref(xn, B_w_gate, B_w_up, B_w_down)
```

```cpp
#include <hip/hip_runtime.h>
#include <hip/hip_bf16.h>

typedef __attribute__((ext_vector_type(8))) __bf16 bf16x8;
typedef __attribute__((ext_vector_type(4))) __bf16 bf16x4;
typedef __attribute__((ext_vector_type(2))) __bf16 bf16x2;
typedef __attribute__((ext_vector_type(4))) short s16x4;
typedef __attribute__((ext_vector_type(8))) short s16x8;
typedef __attribute__((ext_vector_type(4))) float f32x4;
typedef __attribute__((ext_vector_type(2))) float f32x2;
typedef __attribute__((ext_vector_type(16))) float f32x16;
typedef __attribute__((ext_vector_type(4))) unsigned u32x4;
typedef __attribute__((ext_vector_type(2))) unsigned u32x2;
#define LAS __attribute__((address_space(3)))
typedef LAS char* lptr;
typedef unsigned short u16;

#define DI __device__ __forceinline__

constexpr int SEQ = 2048, DM = 640, NH = 32, FF = 1536;
constexpr int KS2 = 68;
constexpr size_t WS_QKV = 0;
constexpr size_t QKV_PLANE = (size_t)SEQ * 8;
constexpr size_t WS_A2 = WS_QKV + 3 * 32 * 5 * QKV_PLANE;
constexpr size_t WS_END = WS_A2 + (size_t)128 * KS2 * 1024;

DI void glds16(const void* g, lptr l) {
  __builtin_amdgcn_global_load_lds((const __attribute__((address_space(1))) unsigned*)g, (LAS unsigned*)l, 16, 0, 0);
}
DI bf16x8 lds_b128(lptr p) { return *(LAS bf16x8*)p; }
DI s16x4 lds_tr(lptr p) { return __builtin_amdgcn_ds_read_tr16_b64_v4i16((LAS s16x4*)p); }
DI bf16x8 cat_tr(s16x4 lo, s16x4 hi) {
  s16x8 v = __builtin_shufflevector(lo, hi, 0, 1, 2, 3, 4, 5, 6, 7);
  return __builtin_bit_cast(bf16x8, v);
}
DI float dot2(unsigned a, unsigned b, float c) {
  return __builtin_amdgcn_fdot2_f32_bf16(__builtin_bit_cast(bf16x2, a), __builtin_bit_cast(bf16x2, b), c, false);
}
DI unsigned pack2(float a, float b) {
  bf16x2 v; v[0] = (__bf16)a; v[1] = (__bf16)b;
  return __builtin_bit_cast(unsigned, v);
}
#define WAITV(n) asm volatile("s_waitcnt vmcnt(" #n ")" ::: "memory")
#define BARRIER() do { asm volatile("s_waitcnt lgkmcnt(0)" ::: "memory"); __builtin_amdgcn_s_barrier(); asm volatile("" ::: "memory"); } while (0)

constexpr int P1_STAGE = 52 * 1024;
constexpr int P1_STATS = 3 * P1_STAGE;
constexpr int P1_LDS = P1_STATS + 3 * 1024;

template <bool ISQ>
DI void p1_tile(const u16* __restrict__ x, const u16* __restrict__ wqkv, const u16* __restrict__ wg,
                const u16* __restrict__ wu, char* __restrict__ ws, int rt, int cg, lptr sm) {
  constexpr int BM = ISQ ? 256 : 160;
  constexpr int MT = ISQ ? 4 : 5, NT = ISQ ? 5 : 4;
  constexpr int NWN = ISQ ? 2 : 4;
  constexpr int APC = BM / 8;
  constexpr int ABYTES = BM * 128;
  const int tid = threadIdx.x, lane = tid & 63;
  const int wid = __builtin_amdgcn_readfirstlane(tid >> 6);
  const int wm = wid / NWN, wn = wid % NWN;
  const int row0 = rt * BM;

  const char* pbase[7];
  unsigned poff[7];
  unsigned padv[7];
#pragma unroll
  for (int i = 0; i < 7; ++i) {
    int p = wid + 8 * i;
    if (p > 51) p = 51;
    if (p < APC) {
      int rl = 8 * p + (lane >> 3), pc = lane & 7;
      int c = pc ^ ((rl >> 1) & 7);
      int gr = row0 + rl; gr = gr > SEQ - 1 ? SEQ - 1 : gr;
      pbase[i] = (const char*)x;
      poff[i] = (unsigned)(gr * DM + c * 8) * 2u;
      padv[i] = 128u;
    } else {
      int pb = p - APC;
      if (ISQ) {
        int ci = pb * 64 + lane;
        int kr = ci / 20, pc = ci - kr * 20;
        int c = pc ^ (((kr >> 3) & 1) << 1);
        pbase[i] = (const char*)wqkv;
        poff[i] = (unsigned)(kr * (3 * DM) + cg * 160 + c * 8) * 2u;
        padv[i] = 64u * 3u * DM * 2u;
      } else {
        int u = pb >> 3, kb = pb & 7;
        int r = lane >> 3, pc = lane & 7;
        int f = (((r >> 1) & 1) << 1) | ((kb & 1) << 2);
        int c = pc ^ f;
        pbase[i] = (const char*)((u < 2) ? wg : wu);
        poff[i] = (unsigned)((kb * 8 + r) * FF + cg * 128 + (u & 1) * 64 + c * 8) * 2u;
        padv[i] = 64u * FF * 2u;
      }
    }
  }
  auto issue = [&](int so) {
#pragma unroll
    for (int i = 0; i < 7; ++i) {
      if (i < 6 || wid < 4) {
        glds16(pbase[i] + poff[i], sm + so + (wid + 8 * i) * 1024);
      }
      pbase[i] += padv[i];
    }
  };

  const int r = lane & 15, g = lane >> 4, q = (lane & 15) >> 2, pp = lane & 3;
  unsigned xa[MT];
#pragma unroll
  for (int i = 0; i < MT; ++i) {
    int tile;
    if (ISQ) tile = (i + 2 * wn) & 3;
    else tile = (i < 4) ? ((i + wn) & 3) : 4;
    xa[i] = (unsigned)((wm * MT * 16 + tile * 16 + r) * 128 + ((g ^ (r >> 1)) * 16));
  }
  unsigned wa[NT];
#pragma unroll
  for (int j = 0; j < NT; ++j) {
    if (ISQ) {
      int jt = (j < 4) ? ((j + wm) & 3) : 4;
      int nt = 5 * wn + jt;
      wa[j] = (unsigned)(ABYTES + (8 * g + q) * 320 + ((nt ^ (g & 1)) * 32) + pp * 8);
    } else {
      int ja = (j + 2 * wm) & 3;
      int u = (ja >> 1) * 2 + (wn >> 1);
      int nt = 2 * (wn & 1) + (ja & 1);
      int f2 = (q >> 1) | ((g & 1) << 1);
      wa[j] = (unsigned)(ABYTES + (u * 8 + g) * 1024 + q * 128 + ((nt ^ f2) * 32) + pp * 8);
    }
  }
  constexpr int W_T = ISQ ? 4 * 320 : 512;
  constexpr int W_S = ISQ ? 32 * 320 : 4096;

  f32x4 acc[MT][NT];
#pragma unroll
  for (int i = 0; i < MT; ++i)
#pragma unroll
    for (int j = 0; j < NT; ++j) acc[i][j] = f32x4{0.f, 0.f, 0.f, 0.f};
  float sx0 = 0.f, sq0 = 0.f, sx1 = 0.f, sq1 = 0.f, cs0 = 0.f, cs1 = 0.f;
  const unsigned ONES = 0x3f803f80u;
  constexpr int XS1 = ISQ ? 1 : 4;
  constexpr int WS1 = ISQ ? 4 : 1;

  issue(0);
  issue(P1_STAGE);
  int so_cur = 0, so_nxt = 2 * P1_STAGE;
#pragma unroll 1
  for (int t = 0; t < 10; ++t) {
    if (t < 9) { if (wid < 4) WAITV(7); else WAITV(6); } else WAITV(0);
    BARRIER();
    if (t + 2 < 10) issue(so_nxt);
    lptr sb = sm + so_cur;
    so_nxt = so_cur;
    so_cur = (so_cur == 2 * P1_STAGE) ? 0 : so_cur + P1_STAGE;
#pragma unroll
    for (int s = 0; s < 2; ++s) {
      bf16x8 xf[MT], wf[NT];
#pragma unroll
      for (int j = 0; j < NT; ++j) {
        s16x4 lo = lds_tr(sb + wa[j] + s * W_S);
        s16x4 hi = lds_tr(sb + wa[j] + s * W_S + W_T);
        wf[j] = cat_tr(lo, hi);
      }
#pragma unroll
      for (int i = 0; i < MT; ++i) xf[i] = lds_b128(sb + (xa[i] ^ (s * 64)));
      {
        u32x4 a = __builtin_bit_cast(u32x4, xf[0]), b = __builtin_bit_cast(u32x4, xf[XS1]);
        u32x4 c = __builtin_bit_cast(u32x4, wf[0]), d = __builtin_bit_cast(u32x4, wf[WS1]);
#pragma unroll
        for (int e = 0; e < 4; ++e) {
          sx0 = dot2(a[e], ONES, sx0); sq0 = dot2(a[e], a[e], sq0);
          sx1 = dot2(b[e], ONES, sx1); sq1 = dot2(b[e], b[e], sq1);
          cs0 = dot2(c[e], ONES, cs0); cs1 = dot2(d[e], ONES, cs1);
        }
      }
#pragma unroll
      for (int i = 0; i < MT; ++i)
#pragma unroll
        for (int j = 0; j < NT; ++j)
          acc[i][j] = __builtin_amdgcn_mfma_f32_16x16x32_bf16(wf[j], xf[i], acc[i][j], 0, 0, 0);
    }
  }

  sx0 += __shfl_xor(sx0, 16); sq0 += __shfl_xor(sq0, 16); sx1 += __shfl_xor(sx1, 16); sq1 += __shfl_xor(sq1, 16);
  cs0 += __shfl_xor(cs0, 16); cs1 += __shfl_xor(cs1, 16);
  sx0 += __shfl_xor(sx0, 32); sq0 += __shfl_xor(sq0, 32); sx1 += __shfl_xor(sx1, 32); sq1 += __shfl_xor(sq1, 32);
  cs0 += __shfl_xor(cs0, 32); cs1 += __shfl_xor(cs1, 32);
  LAS f32x2* rowstat = (LAS f32x2*)(sm + P1_STATS);
  LAS float* colsum = (LAS float*)(sm + P1_STATS + 2048);
  {
    const float inv = 1.0f / DM;
    float m0 = sx0 * inv, m1 = sx1 * inv;
    float v0 = fmaxf(sq0 * inv - m0 * m0, 0.f), v1 = fmaxf(sq1 * inv - m1 * m1, 0.f);
    float r0 = rsqrtf(v0 + 1e-5f), r1 = rsqrtf(v1 + 1e-5f);
    int t0, t1, n0, n1;
    if (ISQ) {
      t0 = wm * 4 + ((2 * wn) & 3); t1 = wm * 4 + ((1 + 2 * wn) & 3);
      n0 = 5 * wn + (wm & 3); n1 = 5 * wn + 4;
    } else {
      t0 = wm * 5 + wn; t1 = wm * 5 + 4;
      n0 = wm * 8 + 2 * wn; n1 = wm * 8 + 2 * wn + 1;
    }
    if (g == 0) {
      rowstat[t0 * 16 + r] = f32x2{m0, r0};
      rowstat[t1 * 16 + r] = f32x2{m1, r1};
      colsum[n0 * 16 + r] = cs0;
      colsum[n1 * 16 + r] = cs1;
    }
  }
  BARRIER();

  f32x4 csv[NT];
#pragma unroll
  for (int j = 0; j < NT; ++j) {
    int nb;
    if (ISQ) { int jt = (j < 4) ? ((j + wm) & 3) : 4; nb = 5 * wn + jt; }
    else { int ja = (j + 2 * wm) & 3; nb = (ja >> 1) * 8 + 2 * wn + (ja & 1); }
    csv[j] = *(LAS f32x4*)(colsum + nb * 16 + 4 * g);
  }
#pragma unroll
  for (int i = 0; i < MT; ++i) {
    int tile;
    if (ISQ) tile = (i + 2 * wn) & 3;
    else tile = (i < 4) ? ((i + wn) & 3) : 4;
    const int ml = wm * MT * 16 + tile * 16 + r;
    const f32x2 st = rowstat[ml];
    const int m = row0 + ml;
    if (ISQ) {
      const int which = cg >> 2;
#pragma unroll
      for (int j = 0; j < NT; ++j) {
        int jt = (j < 4) ? ((j + wm) & 3) : 4;
        int col = (5 * wn + jt) * 16 + 4 * g;
        int hl = col / 20, quad = (col - hl * 20) >> 2;
        int head = (cg & 3) * 8 + hl;
        f32x4 y;
#pragma unroll
        for (int e = 0; e < 4; ++e) y[e] = st[1] * (acc[i][j][e] - st[0] * csv[j][e]);
        u32x2 o2 = {pack2(y[0], y[1]), pack2(y[2], y[3])};
        *(u32x2*)(ws + WS_QKV + (size_t)((which * 32 + head) * 5 + quad) * QKV_PLANE + (size_t)m * 8) = o2;
      }
    } else {
      if (m < SEQ) {
#pragma unroll
        for (int tp = 0; tp < 2; ++tp) {
          f32x4 ya, yb;
#pragma unroll
          for (int e = 0; e < 4; ++e) {
            ya[e] = st[1] * (acc[i][tp][e] - st[0] * csv[tp][e]);
            yb[e] = st[1] * (acc[i][tp + 2][e] - st[0] * csv[tp + 2][e]);
          }
          float hv[4];
#pragma unroll
          for (int e = 0; e < 4; ++e) {
            float gt = wm == 0 ? ya[e] : yb[e];
            float up = wm == 0 ? yb[e] : ya[e];
            float sg = __builtin_amdgcn_rcpf(1.0f + __expf(-gt));
            hv[e] = gt * sg * up;
          }
          u32x2 o2 = {pack2(hv[0], hv[1]), pack2(hv[2], hv[3])};
          int ks = 20 + 4 * cg + wn;
          int gg = 2 * tp + (g >> 1);
          *(u32x2*)(ws + WS_A2 + ((size_t)(m >> 4) * KS2 + ks) * 1024 + (gg * 16 + r) * 16 + (g & 1) * 8) = o2;
        }
      }
    }
  }
}

__global__ __launch_bounds__(512, 2) void k_p1(const u16* __restrict__ x, const u16* __restrict__ wqkv,
                                               const u16* __restrict__ wg, const u16* __restrict__ wu,
                                               char* __restrict__ ws) {
  extern __shared__ __attribute__((aligned(16))) char smem[];
  lptr sm = (lptr)smem;
  const int e = blockIdx.x & 7, idx = blockIdx.x >> 3;
  const int p = e >> 1, half = e & 1;
  if (idx < 12) {
    int rt = half * 4 + idx / 3, cg = 3 * p + idx % 3;
    p1_tile<true>(x, wqkv, wg, wu, ws, rt, cg, sm);
  } else {
    int k = idx - 12;
    int rt, c3;
    if (half == 0) { rt = k / 3; c3 = k % 3; }
    else { if (k == 0) { rt = 6; c3 = 2; } else { rt = 7 + (k - 1) / 3; c3 = (k - 1) % 3; } }
    if (rt > 12) return;
    p1_tile<false>(x, wqkv, wg, wu, ws, rt, 3 * p + c3, sm);
  }
}

constexpr int P2_PSTRIDE = 3072 + 32;
constexpr int P2_LDS = 21 * P2_PSTRIDE + 64;

DI void p2_block(char* __restrict__ ws, int pi, int hp, lptr sm) {
  const int tid = threadIdx.x, lane = tid & 63;
  const int wid = __builtin_amdgcn_readfirstlane(tid >> 6);
  {
    const int kp_min = pi >= 2 ? 0 : 2 - pi;
    for (int pc = wid; pc < 60; pc += 8) {
      int pl = pc / 3, kp = pc - pl * 3;
      if (kp >= kp_min) {
        int hh = pl / 10, w10 = pl - hh * 10;
        int which = 1 + (w10 >= 5), quad = w10 - (w10 >= 5 ? 5 : 0);
        int head = hp * 2 + hh;
        const char* src = ws + WS_QKV + (size_t)((which * 32 + head) * 5 + quad) * QKV_PLANE +
                          (size_t)((pi - 2 + kp) * 128) * 8 + lane * 16;
        glds16(src, sm + pl * P2_PSTRIDE + kp * 1024);
      }
    }
    if (tid < 384) *(LAS u32x2*)(sm + 20 * P2_PSTRIDE + tid * 8) = u32x2{0x00003f80u, 0u};
  }
  const int qt = wid & 3, hh = wid >> 2;
  const int head = hp * 2 + hh;
  const int q0 = pi * 128 + qt * 32;
  const int c = lane & 31, h = lane >> 5;
  bf16x8 qf0, qf1;
  {
    const char* qb = ws + WS_QKV + (size_t)(head * 5) * QKV_PLANE + (size_t)(q0 + c) * 8;
    u32x2 a = *(const u32x2*)(qb + (size_t)(2 * h) * QKV_PLANE);
    u32x2 b = *(const u32x2*)(qb + (size_t)(2 * h + 1) * QKV_PLANE);
    u32x2 d = *(const u32x2*)(qb + (size_t)4 * QKV_PLANE);
    u32x4 v0 = {a[0], a[1], b[0], b[1]};
    u32x4 v1 = {h ? 0u : d[0], h ? 0u : d[1], 0u, 0u};
    qf0 = __builtin_bit_cast(bf16x8, v0);
    qf1 = __builtin_bit_cast(bf16x8, v1);
  }
  WAITV(0);
  BARRIER();

  const int kt_min = q0 >= 256 ? 0 : (256 - q0) >> 5;
  lptr kb = sm + (hh * 10) * P2_PSTRIDE + (qt * 32 + c) * 8;
  f32x16 S[9];
#pragma unroll
  for (int kt = 0; kt < 9; ++kt) {
    if (kt >= kt_min) {
      u32x2 a = *(LAS u32x2*)(kb + (2 * h) * P2_PSTRIDE + kt * 256);
      u32x2 b = *(LAS u32x2*)(kb + (2 * h + 1) * P2_PSTRIDE + kt * 256);
      u32x2 d = *(LAS u32x2*)(kb + 4 * P2_PSTRIDE + kt * 256);
      u32x4 v0 = {a[0], a[1], b[0], b[1]};
      u32x4 v1 = {d[0], d[1], 0u, 0u};
      f32x16 z;
#pragma unroll
      for (int e = 0; e < 16; ++e) z[e] = 0.f;
      z = __builtin_amdgcn_mfma_f32_32x32x16_bf16(__builtin_bit_cast(bf16x8, v0), qf0, z, 0, 0, 0);
      z = __builtin_amdgcn_mfma_f32_32x32x16_bf16(__builtin_bit_cast(bf16x8, v1), qf1, z, 0, 0, 0);
      S[kt] = z;
    } else {
#pragma unroll
      for (int e = 0; e < 16; ++e) S[kt][e] = -INFINITY;
    }
  }
#pragma unroll
  for (int e = 0; e < 16; ++e) {
    int rr = (e & 3) + 8 * (e >> 2) + 4 * h;
    if (!(rr > c)) S[0][e] = -INFINITY;
    if (!(rr <= c)) S[8][e] = -INFINITY;
  }
  float mx = S[8][0];
#pragma unroll
  for (int kt = 0; kt < 9; ++kt)
#pragma unroll
    for (int e = 0; e < 16; ++e) mx = fmaxf(mx, S[kt][e]);
  mx = fmaxf(mx, __shfl_xor(mx, 32));
  const float C2 = 0.22360679774997896f * 1.4426950408889634f;
  const float nm = -mx * C2;

  f32x16 O;
#pragma unroll
  for (int e = 0; e < 16; ++e) O[e] = 0.f;
  {
    const int blk = (lane >> 4) & 1, q = (lane & 15) >> 2, pp = lane & 3;
    int pl = blk ? (pp == 0 ? hh * 10 + 9 : 20) : hh * 10 + 5 + pp;
    lptr vb = sm + pl * P2_PSTRIDE + (qt * 32 + 4 * h + q) * 8;
#pragma unroll
    for (int kt = 0; kt < 9; ++kt) {
      if (kt >= kt_min) {
#pragma unroll
        for (int s = 0; s < 2; ++s) {
          u32x4 pk;
#pragma unroll
          for (int e = 0; e < 4; ++e) {
            float p0 = __builtin_amdgcn_exp2f(__builtin_fmaf(S[kt][8 * s + 2 * e], C2, nm));
            float p1 = __builtin_amdgcn_exp2f(__builtin_fmaf(S[kt][8 * s + 2 * e + 1], C2, nm));
            pk[e] = pack2(p0, p1);
          }
          s16x4 lo = lds_tr(vb + kt * 256 + s * 128);
          s16x4 hi = lds_tr(vb + kt * 256 + s * 128 + 64);
          O = __builtin_amdgcn_mfma_f32_32x32x16_bf16(cat_tr(lo, hi), __builtin_bit_cast(bf16x8, pk), O, 0, 0, 0);
        }
      }
    }
  }
  float l = __shfl(O[8], c + 32);
  float inv = 1.0f / l;
  {
    const int m = q0 + c;
    char* ob = ws + WS_A2 + (size_t)(m >> 4) * KS2 * 1024 + (m & 15) * 16;
#pragma unroll
    for (int grp = 0; grp < 3; ++grp) {
      if (grp < 2 || h == 0) {
        int kidx = head * 20 + 8 * grp + 4 * h;
        u32x2 o2 = {pack2(O[4 * grp] * inv, O[4 * grp + 1] * inv), pack2(O[4 * grp + 2] * inv, O[4 * grp + 3] * inv)};
        *(u32x2*)(ob + (kidx >> 5) * 1024 + ((kidx >> 3) & 3) * 256 + ((kidx >> 2) & 1) * 8) = o2;
      }
    }
  }
}

__global__ __launch_bounds__(512, 2) void k_p2(char* __restrict__ ws) {
  extern __shared__ __attribute__((aligned(16))) char smem[];
  const int e = blockIdx.x & 7, idx = blockIdx.x >> 3;
  p2_block(ws, idx >> 1, e * 2 + (idx & 1), (lptr)smem);
}

constexpr int P3_STAGE = 40 * 1024;
constexpr int P3_LDS = 3 * P3_STAGE;

DI void p3_tile(const u16* __restrict__ x, const u16* __restrict__ wo, const u16* __restrict__ wd,
                const char* __restrict__ ws, u16* __restrict__ out, int rt, int ct, lptr sm) {
  const int tid = threadIdx.x, lane = tid & 63;
  const int wid = __builtin_amdgcn_readfirstlane(tid >> 6);
  const int kh = wid >> 2, mh = (wid >> 1) & 1, nh = wid & 1;
  unsigned aoff[3], boff[2];
#pragma unroll
  for (int i = 0; i < 3; ++i) {
    int p = wid + 8 * i;
    int mt = p >> 2, ks = p & 3;
    int gmt = rt * 6 + mt; gmt = gmt > 127 ? 127 : gmt;
    aoff[i] = (unsigned)((gmt * KS2 + ks) * 1024 + lane * 16);
  }
#pragma unroll
  for (int i = 0; i < 2; ++i) {
    int kb = wid + 8 * i;
    int r = lane >> 3, pc = lane & 7;
    int f = (((r >> 1) & 1) << 1) | ((kb & 1) << 2);
    int c = pc ^ f;
    boff[i] = (unsigned)((kb * 8 + r) * DM + ct * 64 + c * 8) * 2u;
  }
  auto issue = [&](int st, int slot) {
    const char* ab = ws + WS_A2 + (st < 12 ? (20 + 4 * st) : 4 * (st - 12)) * 1024;
    const char* bb = st < 12 ? (const char*)wd + (size_t)st * 128 * DM * 2 : (const char*)wo + (size_t)(st - 12) * 128 * DM * 2;
#pragma unroll
    for (int i = 0; i < 3; ++i) glds16(ab + aoff[i], sm + slot * P3_STAGE + (wid + 8 * i) * 1024);
#pragma unroll
    for (int i = 0; i < 2; ++i) glds16(bb + boff[i], sm + slot * P3_STAGE + (24 + wid + 8 * i) * 1024);
  };
  const int r = lane & 15, g = lane >> 4, q = (lane & 15) >> 2, pp = lane & 3;
  const unsigned xa = (unsigned)((mh * 3 * 4 + kh * 2) * 1024 + lane * 16);
  unsigned wa[2];
#pragma unroll
  for (int j = 0; j < 2; ++j) {
    int nt = 2 * nh + j;
    int f2 = (q >> 1) | ((g & 1) << 1);
    wa[j] = (unsigned)(24 * 1024 + (kh * 8 + g) * 1024 + q * 128 + ((nt ^ f2) * 32) + pp * 8);
  }
  f32x4 acc[3][2];
#pragma unroll
  for (int i = 0; i < 3; ++i)
#pragma unroll
    for (int j = 0; j < 2; ++j) acc[i][j] = f32x4{0.f, 0.f, 0.f, 0.f};

  issue(0, 0);
  issue(1, 1);
#pragma unroll
  for (int t = 0; t < 17; ++t) {
    if (t < 16) WAITV(5); else WAITV(0);
    BARRIER();
    if (t + 2 < 17) issue(t + 2, (t + 2) % 3);
    lptr sb = sm + (t % 3) * P3_STAGE;
#pragma unroll
    for (int s = 0; s < 2; ++s) {
      bf16x8 xf[3], wf[2];
#pragma unroll
      for (int j = 0; j < 2; ++j) {
        s16x4 lo = lds_tr(sb + wa[j] + s * 4096);
        s16x4 hi = lds_tr(sb + wa[j] + s * 4096 + 512);
        wf[j] = cat_tr(lo, hi);
      }
#pragma unroll
      for (int i = 0; i < 3; ++i) xf[i] = lds_b128(sb + xa + i * 4096 + s * 1024);
#pragma unroll
      for (int i = 0; i < 3; ++i)
#pragma unroll
        for (int j = 0; j < 2; ++j)
          acc[i][j] = __builtin_amdgcn_mfma_f32_16x16x32_bf16(wf[j], xf[i], acc[i][j], 0, 0, 0);
    }
  }
  BARRIER();
  constexpr int RS = 68;
  {
    LAS float* red = (LAS float*)sm + kh * 96 * RS;
#pragma unroll
    for (int i = 0; i < 3; ++i)
#pragma unroll
      for (int j = 0; j < 2; ++j)
        *(LAS f32x4*)(red + ((mh * 3 + i) * 16 + r) * RS + (2 * nh + j) * 16 + 4 * g) = acc[i][j];
  }
  BARRIER();
  for (int it = tid; it < 96 * 8; it += 512) {
    int ml = it >> 3, c8 = it & 7;
    int m = rt * 96 + ml;
    if (m < SEQ) {
      LAS float* r0 = (LAS float*)sm + ml * RS + c8 * 8;
      LAS float* r1 = r0 + 96 * RS;
      f32x4 a0 = *(LAS f32x4*)r0, a1 = *(LAS f32x4*)(r0 + 4);
      f32x4 b0 = *(LAS f32x4*)r1, b1 = *(LAS f32x4*)(r1 + 4);
      u32x4 xv = *(const u32x4*)(x + (size_t)m * DM + ct * 64 + c8 * 8);
      float v[8];
#pragma unroll
      for (int e = 0; e < 4; ++e) { v[e] = a0[e] + b0[e]; v[4 + e] = a1[e] + b1[e]; }
      u32x4 o;
#pragma unroll
      for (int e = 0; e < 4; ++e) {
        float lo = __uint_as_float(xv[e] << 16), hi = __uint_as_float(xv[e] & 0xffff0000u);
        o[e] = pack2(lo + v[2 * e], hi + v[2 * e + 1]);
      }
      *(u32x4*)(out + (size_t)m * DM + ct * 64 + c8 * 8) = o;
    }
  }
}

__global__ __launch_bounds__(512, 2) void k_p3(const u16* __restrict__ x, const u16* __restrict__ wo,
                                               const u16* __restrict__ wd, const char* __restrict__ ws,
                                               u16* __restrict__ out) {
  extern __shared__ __attribute__((aligned(16))) char smem[];
  const int e = blockIdx.x & 7, idx = blockIdx.x >> 3;
  const int qd = e >> 1;
  const int start = (qd * 110 + 3) / 4;
  const int end = ((qd + 1) * 110 + 3) / 4;
  const int li = start + idx;
  if (li >= end) return;
  p3_tile(x, wo, wd, ws, out, li / 5, (e & 1) * 5 + li % 5, (lptr)smem);
}

extern "C" void kernel_launch(void* const* d_in, const int* in_sizes, int n_in,
                              void* d_out, int out_size, void* d_ws, size_t ws_size,
                              hipStream_t stream) {
  const u16* x = (const u16*)d_in[0];
  const u16* wqkv = (const u16*)d_in[1];
  const u16* wo = (const u16*)d_in[2];
  const u16* wg = (const u16*)d_in[4];
  const u16* wu = (const u16*)d_in[5];
  const u16* wd = (const u16*)d_in[6];
  char* ws = (char*)d_ws;
  (void)hipFuncSetAttribute((const void*)k_p1, hipFuncAttributeMaxDynamicSharedMemorySize, P1_LDS);
  (void)hipFuncSetAttribute((const void*)k_p2, hipFuncAttributeMaxDynamicSharedMemorySize, P2_LDS);
  (void)hipFuncSetAttribute((const void*)k_p3, hipFuncAttributeMaxDynamicSharedMemorySize, P3_LDS);
  k_p1<<<256, 512, P1_LDS, stream>>>(x, wqkv, wg, wu, ws);
  k_p2<<<256, 512, P2_LDS, stream>>>(ws);
  k_p3<<<224, 512, P3_LDS, stream>>>(x, wo, wd, ws, (u16*)d_out);
}
```

```cpp
#include <hip/hip_runtime.h>
#include <hip/hip_bf16.h>

typedef __attribute__((ext_vector_type(8))) __bf16 bf16x8;
typedef __attribute__((ext_vector_type(4))) __bf16 bf16x4;
typedef __attribute__((ext_vector_type(2))) __bf16 bf16x2;
typedef __attribute__((ext_vector_type(4))) short s16x4;
typedef __attribute__((ext_vector_type(8))) short s16x8;
typedef __attribute__((ext_vector_type(4))) float f32x4;
typedef __attribute__((ext_vector_type(2))) float f32x2;
typedef __attribute__((ext_vector_type(16))) float f32x16;
typedef __attribute__((ext_vector_type(4))) unsigned u32x4;
typedef __attribute__((ext_vector_type(2))) unsigned u32x2;
#define LAS __attribute__((address_space(3)))
typedef LAS char* lptr;
typedef unsigned short u16;

#define DI __device__ __forceinline__

constexpr int SEQ = 2048, DM = 640, NH = 32, FF = 1536;
constexpr int KS2 = 68;
constexpr size_t WS_QKV = 0;
constexpr size_t QKV_PLANE = (size_t)SEQ * 8;
constexpr size_t WS_A2 = WS_QKV + 3 * 32 * 5 * QKV_PLANE;
constexpr size_t WS_END = WS_A2 + (size_t)128 * KS2 * 1024;

DI void glds16(const void* g, lptr l) {
  __builtin_amdgcn_global_load_lds((const __attribute__((address_space(1))) unsigned*)g, (LAS unsigned*)l, 16, 0, 0);
}
DI bf16x8 lds_b128(lptr p) { return *(LAS bf16x8*)p; }
DI s16x4 lds_tr(lptr p) { return __builtin_amdgcn_ds_read_tr16_b64_v4i16((LAS s16x4*)p); }
DI bf16x8 cat_tr(s16x4 lo, s16x4 hi) {
  s16x8 v = __builtin_shufflevector(lo, hi, 0, 1, 2, 3, 4, 5, 6, 7);
  return __builtin_bit_cast(bf16x8, v);
}
DI float dot2(unsigned a, unsigned b, float c) {
  return __builtin_amdgcn_fdot2_f32_bf16(__builtin_bit_cast(bf16x2, a), __builtin_bit_cast(bf16x2, b), c, false);
}
DI unsigned pack2(float a, float b) {
  bf16x2 v; v[0] = (__bf16)a; v[1] = (__bf16)b;
  return __builtin_bit_cast(unsigned, v);
}
#define STAMP(ws, ph, i) do {} while (0)
#define WAITV(n) asm volatile("s_waitcnt vmcnt(" #n ")" ::: "memory")
#define BARRIER() do { asm volatile("" ::: "memory"); __builtin_amdgcn_s_waitcnt(0xC07F); __builtin_amdgcn_s_barrier(); asm volatile("" ::: "memory"); } while (0)
DI u32x4 lds_u128(lptr p) { return *(LAS u32x4*)p; }
DI u32x2 lds_tru(lptr p) { return __builtin_bit_cast(u32x2, __builtin_amdgcn_ds_read_tr16_b64_v4i16((LAS s16x4*)p)); }
DI bf16x8 cat_u(u32x2 lo, u32x2 hi) { u32x4 v = {lo[0], lo[1], hi[0], hi[1]}; return __builtin_bit_cast(bf16x8, v); }

constexpr int P1_STAGE = 52 * 1024;
constexpr int P1_STATS = 3 * P1_STAGE;
constexpr int P1_LDS = P1_STATS + 3 * 1024;

template <bool ISQ>
DI void p1_tile(const u16* __restrict__ x, const u16* __restrict__ wqkv, const u16* __restrict__ wg,
                const u16* __restrict__ wu, char* __restrict__ ws, int rt, int cg, lptr sm) {
  constexpr int BM = ISQ ? 256 : 160;
  constexpr int MT = ISQ ? 4 : 5, NT = ISQ ? 5 : 4;
  constexpr int NWN = ISQ ? 2 : 4;
  constexpr int APC = BM / 8;
  constexpr int ABYTES = BM * 128;
  const int tid = threadIdx.x, lane = tid & 63;
  const int wid = __builtin_amdgcn_readfirstlane(tid >> 6);
  const int wm = wid / NWN, wn = wid % NWN;
  const int row0 = rt * BM;

  const char* pbase[7];
  unsigned poff[7];
  unsigned padv[7];
#pragma unroll
  for (int i = 0; i < 7; ++i) {
    int p = wid + 8 * i;
    if (p > 51) p = 51;
    if (p < APC) {
      int rl = 8 * p + (lane >> 3), pc = lane & 7;
      int c = pc ^ ((rl >> 1) & 7);
      int gr = row0 + rl; gr = gr > SEQ - 1 ? SEQ - 1 : gr;
      pbase[i] = (const char*)x;
      poff[i] = (unsigned)(gr * DM + c * 8) * 2u;
      padv[i] = 128u;
    } else {
      int pb = p - APC;
      if (ISQ) {
        int ci = pb * 64 + lane;
        int kr = ci / 20, pc = ci - kr * 20;
        int c = pc ^ (((kr >> 3) & 1) << 1);
        pbase[i] = (const char*)wqkv;
        poff[i] = (unsigned)(kr * (3 * DM) + cg * 160 + c * 8) * 2u;
        padv[i] = 64u * 3u * DM * 2u;
      } else {
        int u = pb >> 3, kb = pb & 7;
        int r = lane >> 3, pc = lane & 7;
        int f = (((r >> 1) & 1) << 1) | ((kb & 1) << 2);
        int c = pc ^ f;
        pbase[i] = (const char*)((u < 2) ? wg : wu);
        poff[i] = (unsigned)((kb * 8 + r) * FF + cg * 128 + (u & 1) * 64 + c * 8) * 2u;
        padv[i] = 64u * FF * 2u;
      }
    }
  }
  auto issue1 = [&](int i, int so) {
    if (i < 6 || wid < 4) glds16(pbase[i] + poff[i], sm + so + (wid + 8 * i) * 1024);
    pbase[i] += padv[i];
  };
  auto issue = [&](int so) {
#pragma unroll
    for (int i = 0; i < 7; ++i) issue1(i, so);
  };

  const int r = lane & 15, g = lane >> 4, q = (lane & 15) >> 2, pp = lane & 3;
  unsigned xa[MT];
#pragma unroll
  for (int i = 0; i < MT; ++i) {
    int tile;
    if (ISQ) tile = (i + 2 * wn) & 3;
    else tile = (i < 4) ? ((i + wn) & 3) : 4;
    xa[i] = (unsigned)(size_t)sm + (unsigned)((wm * MT * 16 + tile * 16 + r) * 128 + ((g ^ (r >> 1)) * 16));
  }
  unsigned wa[NT];
#pragma unroll
  for (int j = 0; j < NT; ++j) {
    if (ISQ) {
      int jt = (j < 4) ? ((j + wm) & 3) : 4;
      int nt = 5 * wn + jt;
      wa[j] = (unsigned)(size_t)sm + (unsigned)(ABYTES + (8 * g + q) * 320 + ((nt ^ (g & 1)) * 32) + pp * 8);
    } else {
      int ja = (j + 2 * wm) & 3;
      int u = (ja >> 1) * 2 + (wn >> 1);
      int nt = 2 * (wn & 1) + (ja & 1);
      int f2 = (q >> 1) | ((g & 1) << 1);
      wa[j] = (unsigned)(size_t)sm + (unsigned)(ABYTES + (u * 8 + g) * 1024 + q * 128 + ((nt ^ f2) * 32) + pp * 8);
    }
  }
  constexpr int W_T = ISQ ? 4 * 320 : 512;
  constexpr int W_S = ISQ ? 32 * 320 : 4096;

  f32x4 acc[MT][NT];
#pragma unroll
  for (int i = 0; i < MT; ++i)
#pragma unroll
    for (int j = 0; j < NT; ++j) acc[i][j] = f32x4{0.f, 0.f, 0.f, 0.f};
  float sx0 = 0.f, sq0 = 0.f, sx1 = 0.f, sq1 = 0.f, cs0 = 0.f, cs1 = 0.f;
  const unsigned ONES = 0x3f803f80u;
  constexpr int XS1 = ISQ ? 1 : 4;
  constexpr int WS1 = ISQ ? 4 : 1;

  u32x4 xA[MT], xB[MT];
  u32x2 wAl[NT], wAh[NT], wBl[NT], wBh[NT];
  constexpr int NRD = 2 * NT + MT;
  auto rd = [&](int ri, u32x4* xn, u32x2* wl, u32x2* wh, const unsigned* xad, const unsigned* wad, int s) {
    if (ri == 2) { xn[0] = lds_u128((lptr)xad[0]); return; }
    int k = ri < 2 ? ri : ri - 1;
    if (k < 2 * NT) {
      int j = k >> 1;
      u32x2 v = lds_tru((lptr)wad[j] + s * W_S + (k & 1) * W_T);
      if (k & 1) wh[j] = v; else wl[j] = v;
    } else {
      int i = k - 2 * NT + 1;
      xn[i] = lds_u128((lptr)xad[i]);
    }
  };
  auto stat = [&](int o, const u32x4* xc, const u32x2* wl, const u32x2* wh) {
    int e = o / 6, w = o % 6;
    unsigned a = xc[0][e], b = xc[XS1][e];
    unsigned c = (e < 2 ? wl[0] : wh[0])[e & 1];
    unsigned d = (e < 2 ? wl[WS1] : wh[WS1])[e & 1];
    if (w == 0) { sx0 = dot2(a, ONES, sx0); asm volatile("" : "+v"(sx0)); }
    else if (w == 1) { sq0 = dot2(a, a, sq0); asm volatile("" : "+v"(sq0)); }
    else if (w == 2) { sx1 = dot2(b, ONES, sx1); asm volatile("" : "+v"(sx1)); }
    else if (w == 3) { sq1 = dot2(b, b, sq1); asm volatile("" : "+v"(sq1)); }
    else if (w == 4) { cs0 = dot2(c, ONES, cs0); asm volatile("" : "+v"(cs0)); }
    else { cs1 = dot2(d, ONES, cs1); asm volatile("" : "+v"(cs1)); }
  };
  auto kstep = [&](const u32x4* xc, const u32x2* wcl, const u32x2* wch, u32x4* xn, u32x2* wnl, u32x2* wnh,
                   const unsigned* xad, const unsigned* wad, int sn, bool doi, int g0, int g1, int so) {
#pragma unroll
    for (int k = 0; k < 20; ++k) {
      const int i = k / NT, j = k % NT;
      acc[i][j] = __builtin_amdgcn_mfma_f32_16x16x32_bf16(cat_u(wcl[j], wch[j]), __builtin_bit_cast(bf16x8, xc[i]), acc[i][j], 0, 0, 0);
      __builtin_amdgcn_sched_barrier(0);
      if (k < NRD) rd(k, xn, wnl, wnh, xad, wad, sn);
#pragma unroll
      for (int o = (k * 24) / 20; o < ((k + 1) * 24) / 20; ++o) stat(o, xc, wcl, wch);
      if ((k % 5) == 2) { int gi = g0 + k / 5; if (gi < g1) { if (doi) issue1(gi, so); } }
      __builtin_amdgcn_sched_barrier(0);
    }
  };

  STAMP(ws, 0, 0);
  issue(0);
  issue(P1_STAGE);
  issue(2 * P1_STAGE);
  if (wid < 4) WAITV(14); else WAITV(12);
  BARRIER();
  STAMP(ws, 0, 1);
#pragma unroll
  for (int ri = 0; ri < NRD; ++ri) rd(ri, xA, wAl, wAh, xa, wa, 0);
  int so_cur = 0;
  unsigned xa1[MT];
#pragma unroll
  for (int i = 0; i < MT; ++i) xa1[i] = xa[i] ^ 64u;
#pragma unroll 1
  for (int t = 0; t < 10; ++t) {
    const int so_n = (so_cur == 2 * P1_STAGE) ? 0 : so_cur + P1_STAGE;
    const int so_p = (so_cur == 0) ? 2 * P1_STAGE : so_cur - P1_STAGE;
    const int dlt = so_n - so_cur;
    kstep(xA, wAl, wAh, xB, wBl, wBh, xa1, wa, 1, t >= 1 && t < 8, 4, 7, so_p);
#pragma unroll
    for (int i = 0; i < MT; ++i) { xa[i] += dlt; xa1[i] += dlt; }
#pragma unroll
    for (int j = 0; j < NT; ++j) wa[j] += dlt;
    if (t < 8) { if (wid < 4) WAITV(7); else WAITV(6); } else WAITV(0);
    BARRIER();
    STAMP(ws, 0, 2 + t);
    kstep(xB, wBl, wBh, xA, wAl, wAh, xa, wa, 0, t < 7, 0, 4, so_cur);
    so_cur = so_n;
  }

  STAMP(ws, 0, 11);
  sx0 += __shfl_xor(sx0, 16); sq0 += __shfl_xor(sq0, 16); sx1 += __shfl_xor(sx1, 16); sq1 += __shfl_xor(sq1, 16);
  cs0 += __shfl_xor(cs0, 16); cs1 += __shfl_xor(cs1, 16);
  sx0 += __shfl_xor(sx0, 32); sq0 += __shfl_xor(sq0, 32); sx1 += __shfl_xor(sx1, 32); sq1 += __shfl_xor(sq1, 32);
  cs0 += __shfl_xor(cs0, 32); cs1 += __shfl_xor(cs1, 32);
  LAS f32x2* rowstat = (LAS f32x2*)(sm + P1_STATS);
  LAS float* colsum = (LAS float*)(sm + P1_STATS + 2048);
  {
    const float inv = 1.0f / DM;
    float m0 = sx0 * inv, m1 = sx1 * inv;
    float v0 = fmaxf(sq0 * inv - m0 * m0, 0.f), v1 = fmaxf(sq1 * inv - m1 * m1, 0.f);
    float r0 = rsqrtf(v0 + 1e-5f), r1 = rsqrtf(v1 + 1e-5f);
    int t0, t1, n0, n1;
    if (ISQ) {
      t0 = wm * 4 + ((2 * wn) & 3); t1 = wm * 4 + ((1 + 2 * wn) & 3);
      n0 = 5 * wn + (wm & 3); n1 = 5 * wn + 4;
    } else {
      t0 = wm * 5 + wn; t1 = wm * 5 + 4;
      n0 = wm * 8 + 2 * wn; n1 = wm * 8 + 2 * wn + 1;
    }
    if (g == 0) {
      rowstat[t0 * 16 + r] = f32x2{m0, r0};
      rowstat[t1 * 16 + r] = f32x2{m1, r1};
      colsum[n0 * 16 + r] = cs0;
      colsum[n1 * 16 + r] = cs1;
    }
  }
  BARRIER();

  f32x4 csv[NT];
#pragma unroll
  for (int j = 0; j < NT; ++j) {
    int nb;
    if (ISQ) { int jt = (j < 4) ? ((j + wm) & 3) : 4; nb = 5 * wn + jt; }
    else { int ja = (j + 2 * wm) & 3; nb = (ja >> 1) * 8 + 2 * wn + (ja & 1); }
    csv[j] = *(LAS f32x4*)(colsum + nb * 16 + 4 * g);
  }
#pragma unroll
  for (int i = 0; i < MT; ++i) {
    int tile;
    if (ISQ) tile = (i + 2 * wn) & 3;
    else tile = (i < 4) ? ((i + wn) & 3) : 4;
    const int ml = wm * MT * 16 + tile * 16 + r;
    const f32x2 st = rowstat[ml];
    const int m = row0 + ml;
    if (ISQ) {
      const int which = cg >> 2;
#pragma unroll
      for (int j = 0; j < NT; ++j) {
        int jt = (j < 4) ? ((j + wm) & 3) : 4;
        int col = (5 * wn + jt) * 16 + 4 * g;
        int hl = col / 20, quad = (col - hl * 20) >> 2;
        int head = (cg & 3) * 8 + hl;
        f32x4 y;
#pragma unroll
        for (int e = 0; e < 4; ++e) y[e] = st[1] * (acc[i][j][e] - st[0] * csv[j][e]);
        u32x2 o2 = {pack2(y[0], y[1]), pack2(y[2], y[3])};
        *(u32x2*)(ws + WS_QKV + (size_t)((which * 32 + head) * 5 + quad) * QKV_PLANE + (size_t)m * 8) = o2;
      }
    } else {
      if (m < SEQ) {
#pragma unroll
        for (int tp = 0; tp < 2; ++tp) {
          f32x4 ya, yb;
#pragma unroll
          for (int e = 0; e < 4; ++e) {
            ya[e] = st[1] * (acc[i][tp][e] - st[0] * csv[tp][e]);
            yb[e] = st[1] * (acc[i][tp + 2][e] - st[0] * csv[tp + 2][e]);
          }
          float hv[4];
#pragma unroll
          for (int e = 0; e < 4; ++e) {
            float gt = wm == 0 ? ya[e] : yb[e];
            float up = wm == 0 ? yb[e] : ya[e];
            float sg = __builtin_amdgcn_rcpf(1.0f + __expf(-gt));
            hv[e] = gt * sg * up;
          }
          u32x2 o2 = {pack2(hv[0], hv[1]), pack2(hv[2], hv[3])};
          int ks = 20 + 4 * cg + wn;
          int gg = 2 * tp + (g >> 1);
          *(u32x2*)(ws + WS_A2 + ((size_t)(m >> 4) * KS2 + ks) * 1024 + (gg * 16 + r) * 16 + (g & 1) * 8) = o2;
        }
      }
    }
  }
  STAMP(ws, 0, 12);
}

__global__ __launch_bounds__(512, 2) void k_p1(const u16* __restrict__ x, const u16* __restrict__ wqkv,
                                               const u16* __restrict__ wg, const u16* __restrict__ wu,
                                               char* __restrict__ ws) {
  extern __shared__ __attribute__((aligned(16))) char smem[];
  lptr sm = (lptr)smem;
  const int e = blockIdx.x & 7, idx = blockIdx.x >> 3;
  const int p = e >> 1, half = e & 1;
  if (idx < 12) {
    int rt = half * 4 + idx / 3, cg = 3 * p + idx % 3;
    p1_tile<true>(x, wqkv, wg, wu, ws, rt, cg, sm);
  } else {
    int k = idx - 12;
    int rt, c3;
    if (half == 0) { rt = k / 3; c3 = k % 3; }
    else { if (k == 0) { rt = 6; c3 = 2; } else { rt = 7 + (k - 1) / 3; c3 = (k - 1) % 3; } }
    if (rt > 12) return;
    p1_tile<false>(x, wqkv, wg, wu, ws, rt, 3 * p + c3, sm);
  }
}

constexpr int P2_PSTRIDE = 3072 + 32;
constexpr int P2_LDS = 21 * P2_PSTRIDE + 64;

DI void p2_block(char* __restrict__ ws, int pi, int hp, lptr sm) {
  const int tid = threadIdx.x, lane = tid & 63;
  const int wid = __builtin_amdgcn_readfirstlane(tid >> 6);
  STAMP(ws, 1, 0);
  {
    const int kp_min = pi >= 2 ? 0 : 2 - pi;
    for (int pc = wid; pc < 60; pc += 8) {
      int pl = pc / 3, kp = pc - pl * 3;
      if (kp >= kp_min) {
        int hh = pl / 10, w10 = pl - hh * 10;
        int which = 1 + (w10 >= 5), quad = w10 - (w10 >= 5 ? 5 : 0);
        int head = hp * 2 + hh;
        const char* src = ws + WS_QKV + (size_t)((which * 32 + head) * 5 + quad) * QKV_PLANE +
                          (size_t)((pi - 2 + kp) * 128) * 8 + lane * 16;
        glds16(src, sm + pl * P2_PSTRIDE + kp * 1024);
      }
    }
    if (tid < 384) *(LAS u32x2*)(sm + 20 * P2_PSTRIDE + tid * 8) = u32x2{0x00003f80u, 0u};
  }
  const int qt = wid & 3, hh = wid >> 2;
  const int head = hp * 2 + hh;
  const int q0 = pi * 128 + qt * 32;
  const int c = lane & 31, h = lane >> 5;
  bf16x8 qf0, qf1;
  {
    const char* qb = ws + WS_QKV + (size_t)(head * 5) * QKV_PLANE + (size_t)(q0 + c) * 8;
    u32x2 a = *(const u32x2*)(qb + (size_t)(2 * h) * QKV_PLANE);
    u32x2 b = *(const u32x2*)(qb + (size_t)(2 * h + 1) * QKV_PLANE);
    u32x2 d = *(const u32x2*)(qb + (size_t)4 * QKV_PLANE);
    u32x4 v0 = {a[0], a[1], b[0], b[1]};
    u32x4 v1 = {h ? 0u : d[0], h ? 0u : d[1], 0u, 0u};
    qf0 = __builtin_bit_cast(bf16x8, v0);
    qf1 = __builtin_bit_cast(bf16x8, v1);
  }
  WAITV(0);
  BARRIER();
  STAMP(ws, 1, 1);

  const int kt_min = q0 >= 256 ? 0 : (256 - q0) >> 5;
  lptr kb = sm + (hh * 10) * P2_PSTRIDE + (qt * 32 + c) * 8;
  f32x16 S[9];
#pragma unroll
  for (int kt = 0; kt < 9; ++kt) {
    if (kt >= kt_min) {
      u32x2 a = *(LAS u32x2*)(kb + (2 * h) * P2_PSTRIDE + kt * 256);
      u32x2 b = *(LAS u32x2*)(kb + (2 * h + 1) * P2_PSTRIDE + kt * 256);
      u32x2 d = *(LAS u32x2*)(kb + 4 * P2_PSTRIDE + kt * 256);
      u32x4 v0 = {a[0], a[1], b[0], b[1]};
      u32x4 v1 = {d[0], d[1], 0u, 0u};
      f32x16 z;
#pragma unroll
      for (int e = 0; e < 16; ++e) z[e] = 0.f;
      z = __builtin_amdgcn_mfma_f32_32x32x16_bf16(__builtin_bit_cast(bf16x8, v0), qf0, z, 0, 0, 0);
      z = __builtin_amdgcn_mfma_f32_32x32x16_bf16(__builtin_bit_cast(bf16x8, v1), qf1, z, 0, 0, 0);
      S[kt] = z;
    } else {
#pragma unroll
      for (int e = 0; e < 16; ++e) S[kt][e] = -INFINITY;
    }
  }
#pragma unroll
  for (int e = 0; e < 16; ++e) {
    int rr = (e & 3) + 8 * (e >> 2) + 4 * h;
    if (!(rr > c)) S[0][e] = -INFINITY;
    if (!(rr <= c)) S[8][e] = -INFINITY;
  }
  float mx = S[8][0];
#pragma unroll
  for (int kt = 0; kt < 9; ++kt)
#pragma unroll
    for (int e = 0; e < 16; ++e) mx = fmaxf(mx, S[kt][e]);
  mx = fmaxf(mx, __shfl_xor(mx, 32));
  STAMP(ws, 1, 2);
  const float C2 = 0.22360679774997896f * 1.4426950408889634f;
  const float nm = -mx * C2;

  f32x16 O;
#pragma unroll
  for (int e = 0; e < 16; ++e) O[e] = 0.f;
  {
    const int blk = (lane >> 4) & 1, q = (lane & 15) >> 2, pp = lane & 3;
    int pl = blk ? (pp == 0 ? hh * 10 + 9 : 20) : hh * 10 + 5 + pp;
    lptr vb = sm + pl * P2_PSTRIDE + (qt * 32 + 4 * h + q) * 8;
#pragma unroll
    for (int kt = 0; kt < 9; ++kt) {
      if (kt >= kt_min) {
#pragma unroll
        for (int s = 0; s < 2; ++s) {
          u32x4 pk;
#pragma unroll
          for (int e = 0; e < 4; ++e) {
            float p0 = __builtin_amdgcn_exp2f(__builtin_fmaf(S[kt][8 * s + 2 * e], C2, nm));
            float p1 = __builtin_amdgcn_exp2f(__builtin_fmaf(S[kt][8 * s + 2 * e + 1], C2, nm));
            pk[e] = pack2(p0, p1);
          }
          s16x4 lo = lds_tr(vb + kt * 256 + s * 128);
          s16x4 hi = lds_tr(vb + kt * 256 + s * 128 + 64);
          O = __builtin_amdgcn_mfma_f32_32x32x16_bf16(cat_tr(lo, hi), __builtin_bit_cast(bf16x8, pk), O, 0, 0, 0);
        }
      }
    }
  }
  STAMP(ws, 1, 3);
  float l = __shfl(O[8], c + 32);
  float inv = 1.0f / l;
  {
    const int m = q0 + c;
    char* ob = ws + WS_A2 + (size_t)(m >> 4) * KS2 * 1024 + (m & 15) * 16;
#pragma unroll
    for (int grp = 0; grp < 3; ++grp) {
      if (grp < 2 || h == 0) {
        int kidx = head * 20 + 8 * grp + 4 * h;
        u32x2 o2 = {pack2(O[4 * grp] * inv, O[4 * grp + 1] * inv), pack2(O[4 * grp + 2] * inv, O[4 * grp + 3] * inv)};
        *(u32x2*)(ob + (kidx >> 5) * 1024 + ((kidx >> 3) & 3) * 256 + ((kidx >> 2) & 1) * 8) = o2;
      }
    }
  }
  STAMP(ws, 1, 4);
}

__global__ __launch_bounds__(512, 2) void k_p2(char* __restrict__ ws) {
  extern __shared__ __attribute__((aligned(16))) char smem[];
  const int e = blockIdx.x & 7, idx = blockIdx.x >> 3;
  p2_block(ws, idx >> 1, e * 2 + (idx & 1), (lptr)smem);
}

constexpr int P3_STAGE = 40 * 1024;
constexpr int P3_LDS = 3 * P3_STAGE;

DI void p3_tile(const u16* __restrict__ x, const u16* __restrict__ wo, const u16* __restrict__ wd,
                const char* __restrict__ ws, u16* __restrict__ out, int rt, int ct, lptr sm) {
  const int tid = threadIdx.x, lane = tid & 63;
  const int wid = __builtin_amdgcn_readfirstlane(tid >> 6);
  const int kh = wid >> 2, mh = (wid >> 1) & 1, nh = wid & 1;
  unsigned aoff[3], boff[2];
#pragma unroll
  for (int i = 0; i < 3; ++i) {
    int p = wid + 8 * i;
    int mt = p >> 2, ks = p & 3;
    int gmt = rt * 6 + mt; gmt = gmt > 127 ? 127 : gmt;
    aoff[i] = (unsigned)((gmt * KS2 + ks) * 1024 + lane * 16);
  }
#pragma unroll
  for (int i = 0; i < 2; ++i) {
    int kb = wid + 8 * i;
    int r = lane >> 3, pc = lane & 7;
    int f = (((r >> 1) & 1) << 1) | ((kb & 1) << 2);
    int c = pc ^ f;
    boff[i] = (unsigned)((kb * 8 + r) * DM + ct * 64 + c * 8) * 2u;
  }
  auto issue = [&](int st, int slot) {
    const char* ab = ws + WS_A2 + (st < 12 ? (20 + 4 * st) : 4 * (st - 12)) * 1024;
    const char* bb = st < 12 ? (const char*)wd + (size_t)st * 128 * DM * 2 : (const char*)wo + (size_t)(st - 12) * 128 * DM * 2;
#pragma unroll
    for (int i = 0; i < 3; ++i) glds16(ab + aoff[i], sm + slot * P3_STAGE + (wid + 8 * i) * 1024);
#pragma unroll
    for (int i = 0; i < 2; ++i) glds16(bb + boff[i], sm + slot * P3_STAGE + (24 + wid + 8 * i) * 1024);
  };
  const int r = lane & 15, g = lane >> 4, q = (lane & 15) >> 2, pp = lane & 3;
  const unsigned xa = (unsigned)((mh * 3 * 4 + kh * 2) * 1024 + lane * 16);
  unsigned wa[2];
#pragma unroll
  for (int j = 0; j < 2; ++j) {
    int nt = 2 * nh + j;
    int f2 = (q >> 1) | ((g & 1) << 1);
    wa[j] = (unsigned)(24 * 1024 + (kh * 8 + g) * 1024 + q * 128 + ((nt ^ f2) * 32) + pp * 8);
  }
  f32x4 acc[3][2];
#pragma unroll
  for (int i = 0; i < 3; ++i)
#pragma unroll
    for (int j = 0; j < 2; ++j) acc[i][j] = f32x4{0.f, 0.f, 0.f, 0.f};

  STAMP((char*)ws, 2, 0);
  issue(0, 0);
  issue(1, 1);
#pragma unroll
  for (int t = 0; t < 17; ++t) {
    if (t < 16) WAITV(5); else WAITV(0);
    BARRIER();
    if (t == 0 || t == 4 || t == 8 || t == 12 || t == 16) STAMP((char*)ws, 2, 1 + t / 4);
    if (t + 2 < 17) issue(t + 2, (t + 2) % 3);
    lptr sb = sm + (t % 3) * P3_STAGE;
#pragma unroll
    for (int s = 0; s < 2; ++s) {
      bf16x8 xf[3], wf[2];
#pragma unroll
      for (int j = 0; j < 2; ++j) {
        s16x4 lo = lds_tr(sb + wa[j] + s * 4096);
        s16x4 hi = lds_tr(sb + wa[j] + s * 4096 + 512);
        wf[j] = cat_tr(lo, hi);
      }
#pragma unroll
      for (int i = 0; i < 3; ++i) xf[i] = lds_b128(sb + xa + i * 4096 + s * 1024);
#pragma unroll
      for (int i = 0; i < 3; ++i)
#pragma unroll
        for (int j = 0; j < 2; ++j)
          acc[i][j] = __builtin_amdgcn_mfma_f32_16x16x32_bf16(wf[j], xf[i], acc[i][j], 0, 0, 0);
    }
  }
  BARRIER();
  STAMP((char*)ws, 2, 6);
  constexpr int RS = 68;
  {
    LAS float* red = (LAS float*)sm + kh * 96 * RS;
#pragma unroll
    for (int i = 0; i < 3; ++i)
#pragma unroll
      for (int j = 0; j < 2; ++j)
        *(LAS f32x4*)(red + ((mh * 3 + i) * 16 + r) * RS + (2 * nh + j) * 16 + 4 * g) = acc[i][j];
  }
  BARRIER();
  for (int it = tid; it < 96 * 8; it += 512) {
    int ml = it >> 3, c8 = it & 7;
    int m = rt * 96 + ml;
    if (m < SEQ) {
      LAS float* r0 = (LAS float*)sm + ml * RS + c8 * 8;
      LAS float* r1 = r0 + 96 * RS;
      f32x4 a0 = *(LAS f32x4*)r0, a1 = *(LAS f32x4*)(r0 + 4);
      f32x4 b0 = *(LAS f32x4*)r1, b1 = *(LAS f32x4*)(r1 + 4);
      u32x4 xv = *(const u32x4*)(x + (size_t)m * DM + ct * 64 + c8 * 8);
      float v[8];
#pragma unroll
      for (int e = 0; e < 4; ++e) { v[e] = a0[e] + b0[e]; v[4 + e] = a1[e] + b1[e]; }
      u32x4 o;
#pragma unroll
      for (int e = 0; e < 4; ++e) {
        float lo = __uint_as_float(xv[e] << 16), hi = __uint_as_float(xv[e] & 0xffff0000u);
        o[e] = pack2(lo + v[2 * e], hi + v[2 * e + 1]);
      }
      *(u32x4*)(out + (size_t)m * DM + ct * 64 + c8 * 8) = o;
    }
  }
  STAMP((char*)ws, 2, 7);
}

__global__ __launch_bounds__(512, 2) void k_p3(const u16* __restrict__ x, const u16* __restrict__ wo,
                                               const u16* __restrict__ wd, const char* __restrict__ ws,
                                               u16* __restrict__ out) {
  extern __shared__ __attribute__((aligned(16))) char smem[];
  const int e = blockIdx.x & 7, idx = blockIdx.x >> 3;
  const int qd = e >> 1;
  const int start = (qd * 110 + 3) / 4;
  const int end = ((qd + 1) * 110 + 3) / 4;
  const int li = start + idx;
  if (li >= end) return;
  p3_tile(x, wo, wd, ws, out, li / 5, (e & 1) * 5 + li % 5, (lptr)smem);
}

extern "C" void kernel_launch(void* const* d_in, const int* in_sizes, int n_in,
                              void* d_out, int out_size, void* d_ws, size_t ws_size,
                              hipStream_t stream) {
  const u16* x = (const u16*)d_in[0];
  const u16* wqkv = (const u16*)d_in[1];
  const u16* wo = (const u16*)d_in[2];
  const u16* wg = (const u16*)d_in[4];
  const u16* wu = (const u16*)d_in[5];
  const u16* wd = (const u16*)d_in[6];
  char* ws = (char*)d_ws;
  (void)hipFuncSetAttribute((const void*)k_p1, hipFuncAttributeMaxDynamicSharedMemorySize, P1_LDS);
  (void)hipFuncSetAttribute((const void*)k_p2, hipFuncAttributeMaxDynamicSharedMemorySize, P2_LDS);
  (void)hipFuncSetAttribute((const void*)k_p3, hipFuncAttributeMaxDynamicSharedMemorySize, P3_LDS);
  k_p1<<<256, 512, P1_LDS, stream>>>(x, wqkv, wg, wu, ws);
  k_p2<<<256, 512, P2_LDS, stream>>>(ws);
  k_p3<<<224, 512, P3_LDS, stream>>>(x, wo, wd, ws, (u16*)d_out);
}
```

```cpp
#include <hip/hip_runtime.h>
#include <hip/hip_bf16.h>

typedef __attribute__((ext_vector_type(8))) __bf16 bf16x8;
typedef __attribute__((ext_vector_type(4))) __bf16 bf16x4;
typedef __attribute__((ext_vector_type(2))) __bf16 bf16x2;
typedef __attribute__((ext_vector_type(4))) short s16x4;
typedef __attribute__((ext_vector_type(8))) short s16x8;
typedef __attribute__((ext_vector_type(4))) float f32x4;
typedef __attribute__((ext_vector_type(2))) float f32x2;
typedef __attribute__((ext_vector_type(16))) float f32x16;
typedef __attribute__((ext_vector_type(4))) unsigned u32x4;
typedef __attribute__((ext_vector_type(2))) unsigned u32x2;
#define LAS __attribute__((address_space(3)))
typedef LAS char* lptr;
typedef unsigned short u16;

#define DI __device__ __forceinline__
extern "C" __device__ unsigned long long hw_dispatch_id() __asm("llvm.amdgcn.dispatch.id");

constexpr int SEQ = 2048, DM = 640, NH = 32, FF = 1536;
constexpr int KS2 = 68;
constexpr size_t WS_QKV = 0;
constexpr size_t QKV_PLANE = (size_t)SEQ * 8;
constexpr size_t WS_A2 = WS_QKV + 3 * 32 * 5 * QKV_PLANE;
constexpr size_t WS_END = WS_A2 + (size_t)128 * KS2 * 1024;

DI void glds16(const void* g, lptr l) {
  __builtin_amdgcn_global_load_lds((const __attribute__((address_space(1))) unsigned*)g, (LAS unsigned*)l, 16, 0, 0);
}
DI bf16x8 lds_b128(lptr p) { return *(LAS bf16x8*)p; }
DI s16x4 lds_tr(lptr p) { return __builtin_amdgcn_ds_read_tr16_b64_v4i16((LAS s16x4*)p); }
DI bf16x8 cat_tr(s16x4 lo, s16x4 hi) {
  s16x8 v = __builtin_shufflevector(lo, hi, 0, 1, 2, 3, 4, 5, 6, 7);
  return __builtin_bit_cast(bf16x8, v);
}
DI float dot2(unsigned a, unsigned b, float c) {
  return __builtin_amdgcn_fdot2_f32_bf16(__builtin_bit_cast(bf16x2, a), __builtin_bit_cast(bf16x2, b), c, false);
}
DI unsigned pack2(float a, float b) {
  bf16x2 v; v[0] = (__bf16)a; v[1] = (__bf16)b;
  return __builtin_bit_cast(unsigned, v);
}
#define STAMP(ws, ph, i) do {} while (0)
#define WAITV(n) asm volatile("s_waitcnt vmcnt(" #n ")" ::: "memory")
#define BARRIER() do { asm volatile("" ::: "memory"); __builtin_amdgcn_s_waitcnt(0xC07F); __builtin_amdgcn_s_barrier(); asm volatile("" ::: "memory"); } while (0)
DI u32x4 lds_u128(lptr p) { return *(LAS u32x4*)p; }
DI u32x2 lds_tru(lptr p) { return __builtin_bit_cast(u32x2, __builtin_amdgcn_ds_read_tr16_b64_v4i16((LAS s16x4*)p)); }
DI bf16x8 cat_u(u32x2 lo, u32x2 hi) { u32x4 v = {lo[0], lo[1], hi[0], hi[1]}; return __builtin_bit_cast(bf16x8, v); }

constexpr int P1_STAGE = 52 * 1024;
constexpr int P1_STATS = 3 * P1_STAGE;
constexpr int P1_LDS = P1_STATS + 3 * 1024;

template <bool ISQ>
DI void p1_tile(const u16* __restrict__ x, const u16* __restrict__ wqkv, const u16* __restrict__ wg,
                const u16* __restrict__ wu, char* __restrict__ ws, int rt, int cg, lptr sm) {
  constexpr int BM = ISQ ? 256 : 160;
  constexpr int MT = ISQ ? 4 : 5, NT = ISQ ? 5 : 4;
  constexpr int NWN = ISQ ? 2 : 4;
  constexpr int APC = BM / 8;
  constexpr int ABYTES = BM * 128;
  const int tid = threadIdx.x, lane = tid & 63;
  const int wid = __builtin_amdgcn_readfirstlane(tid >> 6);
  const int wm = wid / NWN, wn = wid % NWN;
  const int row0 = rt * BM;

  const char* pbase[7];
  unsigned poff[7];
  unsigned padv[7];
#pragma unroll
  for (int i = 0; i < 7; ++i) {
    int p = wid + 8 * i;
    if (p > 51) p = 51;
    if (p < APC) {
      int rl = 8 * p + (lane >> 3), pc = lane & 7;
      int c = pc ^ ((rl >> 1) & 7);
      int gr = row0 + rl; gr = gr > SEQ - 1 ? SEQ - 1 : gr;
      pbase[i] = (const char*)x;
      poff[i] = (unsigned)(gr * DM + c * 8) * 2u;
      padv[i] = 128u;
    } else {
      int pb = p - APC;
      if (ISQ) {
        int ci = pb * 64 + lane;
        int kr = ci / 20, pc = ci - kr * 20;
        int c = pc ^ (((kr >> 3) & 1) << 1);
        pbase[i] = (const char*)wqkv;
        poff[i] = (unsigned)(kr * (3 * DM) + cg * 160 + c * 8) * 2u;
        padv[i] = 64u * 3u * DM * 2u;
      } else {
        int u = pb >> 3, kb = pb & 7;
        int r = lane >> 3, pc = lane & 7;
        int f = (((r >> 1) & 1) << 1) | ((kb & 1) << 2);
        int c = pc ^ f;
        pbase[i] = (const char*)((u < 2) ? wg : wu);
        poff[i] = (unsigned)((kb * 8 + r) * FF + cg * 128 + (u & 1) * 64 + c * 8) * 2u;
        padv[i] = 64u * FF * 2u;
      }
    }
  }
  auto issue1 = [&](int i, int so) {
    if (i < 6 || wid < 4) glds16(pbase[i] + poff[i], sm + so + (wid + 8 * i) * 1024);
    pbase[i] += padv[i];
  };
  auto issue = [&](int so) {
#pragma unroll
    for (int i = 0; i < 7; ++i) issue1(i, so);
  };

  const int r = lane & 15, g = lane >> 4, q = (lane & 15) >> 2, pp = lane & 3;
  unsigned xa[MT];
#pragma unroll
  for (int i = 0; i < MT; ++i) {
    int tile;
    if (ISQ) tile = (i + 2 * wn) & 3;
    else tile = (i < 4) ? ((i + wn) & 3) : 4;
    xa[i] = (unsigned)(size_t)sm + (unsigned)((wm * MT * 16 + tile * 16 + r) * 128 + ((g ^ (r >> 1)) * 16));
  }
  unsigned wa[NT];
#pragma unroll
  for (int j = 0; j < NT; ++j) {
    if (ISQ) {
      int jt = (j < 4) ? ((j + wm) & 3) : 4;
      int nt = 5 * wn + jt;
      wa[j] = (unsigned)(size_t)sm + (unsigned)(ABYTES + (8 * g + q) * 320 + ((nt ^ (g & 1)) * 32) + pp * 8);
    } else {
      int ja = (j + 2 * wm) & 3;
      int u = (ja >> 1) * 2 + (wn >> 1);
      int nt = 2 * (wn & 1) + (ja & 1);
      int f2 = (q >> 1) | ((g & 1) << 1);
      wa[j] = (unsigned)(size_t)sm + (unsigned)(ABYTES + (u * 8 + g) * 1024 + q * 128 + ((nt ^ f2) * 32) + pp * 8);
    }
  }
  constexpr int W_T = ISQ ? 4 * 320 : 512;
  constexpr int W_S = ISQ ? 32 * 320 : 4096;

  f32x4 acc[MT][NT];
#pragma unroll
  for (int i = 0; i < MT; ++i)
#pragma unroll
    for (int j = 0; j < NT; ++j) acc[i][j] = f32x4{0.f, 0.f, 0.f, 0.f};
  float sx0 = 0.f, sq0 = 0.f, sx1 = 0.f, sq1 = 0.f, cs0 = 0.f, cs1 = 0.f;
  const unsigned ONES = 0x3f803f80u;
  constexpr int XS1 = ISQ ? 1 : 4;
  constexpr int WS1 = ISQ ? 4 : 1;

  u32x4 xA[MT], xB[MT];
  u32x2 wAl[NT], wAh[NT], wBl[NT], wBh[NT];
  constexpr int NRD = 2 * NT + MT;
  auto rd = [&](int ri, u32x4* xn, u32x2* wl, u32x2* wh, const unsigned* xad, const unsigned* wad, int s) {
    if (ri == 2) { xn[0] = lds_u128((lptr)xad[0]); return; }
    int k = ri < 2 ? ri : ri - 1;
    if (k < 2 * NT) {
      int j = k >> 1;
      u32x2 v = lds_tru((lptr)wad[j] + s * W_S + (k & 1) * W_T);
      if (k & 1) wh[j] = v; else wl[j] = v;
    } else {
      int i = k - 2 * NT + 1;
      xn[i] = lds_u128((lptr)xad[i]);
    }
  };
  auto stat = [&](int o, const u32x4* xc, const u32x2* wl, const u32x2* wh) {
    int e = o / 6, w = o % 6;
    unsigned a = xc[0][e], b = xc[XS1][e];
    unsigned c = (e < 2 ? wl[0] : wh[0])[e & 1];
    unsigned d = (e < 2 ? wl[WS1] : wh[WS1])[e & 1];
    if (w == 0) { sx0 = dot2(a, ONES, sx0); asm volatile("" : "+v"(sx0)); }
    else if (w == 1) { sq0 = dot2(a, a, sq0); asm volatile("" : "+v"(sq0)); }
    else if (w == 2) { sx1 = dot2(b, ONES, sx1); asm volatile("" : "+v"(sx1)); }
    else if (w == 3) { sq1 = dot2(b, b, sq1); asm volatile("" : "+v"(sq1)); }
    else if (w == 4) { cs0 = dot2(c, ONES, cs0); asm volatile("" : "+v"(cs0)); }
    else { cs1 = dot2(d, ONES, cs1); asm volatile("" : "+v"(cs1)); }
  };
  auto kstep = [&](const u32x4* xc, const u32x2* wcl, const u32x2* wch, u32x4* xn, u32x2* wnl, u32x2* wnh,
                   const unsigned* xad, const unsigned* wad, int sn, bool doi, int g0, int g1, int so) {
#pragma unroll
    for (int k = 0; k < 20; ++k) {
      const int i = k / NT, j = k % NT;
      acc[i][j] = __builtin_amdgcn_mfma_f32_16x16x32_bf16(cat_u(wcl[j], wch[j]), __builtin_bit_cast(bf16x8, xc[i]), acc[i][j], 0, 0, 0);
      __builtin_amdgcn_sched_barrier(0);
      if (k < NRD) rd(k, xn, wnl, wnh, xad, wad, sn);
#pragma unroll
      for (int o = (k * 24) / 20; o < ((k + 1) * 24) / 20; ++o) stat(o, xc, wcl, wch);
      if ((k % 5) == 2) { int gi = g0 + k / 5; if (gi < g1) { if (doi) issue1(gi, so); } }
      __builtin_amdgcn_sched_barrier(0);
    }
  };

  STAMP(ws, 0, 0);
  issue(0);
  issue(P1_STAGE);
  issue(2 * P1_STAGE);
  if (wid < 4) WAITV(14); else WAITV(12);
  BARRIER();
  STAMP(ws, 0, 1);
#pragma unroll
  for (int ri = 0; ri < NRD; ++ri) rd(ri, xA, wAl, wAh, xa, wa, 0);
  int so_cur = 0;
  unsigned xa1[MT];
#pragma unroll
  for (int i = 0; i < MT; ++i) xa1[i] = xa[i] ^ 64u;
#pragma unroll 1
  for (int t = 0; t < 10; ++t) {
    const int so_n = (so_cur == 2 * P1_STAGE) ? 0 : so_cur + P1_STAGE;
    const int so_p = (so_cur == 0) ? 2 * P1_STAGE : so_cur - P1_STAGE;
    const int dlt = so_n - so_cur;
    kstep(xA, wAl, wAh, xB, wBl, wBh, xa1, wa, 1, t >= 1 && t < 8, 4, 7, so_p);
#pragma unroll
    for (int i = 0; i < MT; ++i) { xa[i] += dlt; xa1[i] += dlt; }
#pragma unroll
    for (int j = 0; j < NT; ++j) wa[j] += dlt;
    if (t < 8) { if (wid < 4) WAITV(7); else WAITV(6); } else WAITV(0);
    BARRIER();
    STAMP(ws, 0, 2 + t);
    kstep(xB, wBl, wBh, xA, wAl, wAh, xa, wa, 0, t < 7, 0, 4, so_cur);
    so_cur = so_n;
  }

  STAMP(ws, 0, 11);
  sx0 += __shfl_xor(sx0, 16); sq0 += __shfl_xor(sq0, 16); sx1 += __shfl_xor(sx1, 16); sq1 += __shfl_xor(sq1, 16);
  cs0 += __shfl_xor(cs0, 16); cs1 += __shfl_xor(cs1, 16);
  sx0 += __shfl_xor(sx0, 32); sq0 += __shfl_xor(sq0, 32); sx1 += __shfl_xor(sx1, 32); sq1 += __shfl_xor(sq1, 32);
  cs0 += __shfl_xor(cs0, 32); cs1 += __shfl_xor(cs1, 32);
  LAS f32x2* rowstat = (LAS f32x2*)(sm + P1_STATS);
  LAS float* colsum = (LAS float*)(sm + P1_STATS + 2048);
  {
    const float inv = 1.0f / DM;
    float m0 = sx0 * inv, m1 = sx1 * inv;
    float v0 = fmaxf(sq0 * inv - m0 * m0, 0.f), v1 = fmaxf(sq1 * inv - m1 * m1, 0.f);
    float r0 = rsqrtf(v0 + 1e-5f), r1 = rsqrtf(v1 + 1e-5f);
    int t0, t1, n0, n1;
    if (ISQ) {
      t0 = wm * 4 + ((2 * wn) & 3); t1 = wm * 4 + ((1 + 2 * wn) & 3);
      n0 = 5 * wn + (wm & 3); n1 = 5 * wn + 4;
    } else {
      t0 = wm * 5 + wn; t1 = wm * 5 + 4;
      n0 = wm * 8 + 2 * wn; n1 = wm * 8 + 2 * wn + 1;
    }
    if (g == 0) {
      rowstat[t0 * 16 + r] = f32x2{m0, r0};
      rowstat[t1 * 16 + r] = f32x2{m1, r1};
      colsum[n0 * 16 + r] = cs0;
      colsum[n1 * 16 + r] = cs1;
    }
  }
  BARRIER();

  f32x4 csv[NT];
#pragma unroll
  for (int j = 0; j < NT; ++j) {
    int nb;
    if (ISQ) { int jt = (j < 4) ? ((j + wm) & 3) : 4; nb = 5 * wn + jt; }
    else { int ja = (j + 2 * wm) & 3; nb = (ja >> 1) * 8 + 2 * wn + (ja & 1); }
    csv[j] = *(LAS f32x4*)(colsum + nb * 16 + 4 * g);
  }
#pragma unroll
  for (int i = 0; i < MT; ++i) {
    int tile;
    if (ISQ) tile = (i + 2 * wn) & 3;
    else tile = (i < 4) ? ((i + wn) & 3) : 4;
    const int ml = wm * MT * 16 + tile * 16 + r;
    const f32x2 st = rowstat[ml];
    const int m = row0 + ml;
    if (ISQ) {
      const int which = cg >> 2;
#pragma unroll
      for (int j = 0; j < NT; ++j) {
        int jt = (j < 4) ? ((j + wm) & 3) : 4;
        int col = (5 * wn + jt) * 16 + 4 * g;
        int hl = col / 20, quad = (col - hl * 20) >> 2;
        int head = (cg & 3) * 8 + hl;
        f32x4 y;
#pragma unroll
        for (int e = 0; e < 4; ++e) y[e] = st[1] * (acc[i][j][e] - st[0] * csv[j][e]);
        u32x2 o2 = {pack2(y[0], y[1]), pack2(y[2], y[3])};
        *(u32x2*)(ws + WS_QKV + (size_t)((which * 32 + head) * 5 + quad) * QKV_PLANE + (size_t)m * 8) = o2;
      }
    } else {
      if (m < SEQ) {
#pragma unroll
        for (int tp = 0; tp < 2; ++tp) {
          f32x4 ya, yb;
#pragma unroll
          for (int e = 0; e < 4; ++e) {
            ya[e] = st[1] * (acc[i][tp][e] - st[0] * csv[tp][e]);
            yb[e] = st[1] * (acc[i][tp + 2][e] - st[0] * csv[tp + 2][e]);
          }
          float hv[4];
#pragma unroll
          for (int e = 0; e < 4; ++e) {
            float gt = wm == 0 ? ya[e] : yb[e];
            float up = wm == 0 ? yb[e] : ya[e];
            float sg = __builtin_amdgcn_rcpf(1.0f + __expf(-gt));
            hv[e] = gt * sg * up;
          }
          u32x2 o2 = {pack2(hv[0], hv[1]), pack2(hv[2], hv[3])};
          int ks = 20 + 4 * cg + wn;
          int gg = 2 * tp + (g >> 1);
          *(u32x2*)(ws + WS_A2 + ((size_t)(m >> 4) * KS2 + ks) * 1024 + (gg * 16 + r) * 16 + (g & 1) * 8) = o2;
        }
      }
    }
  }
  STAMP(ws, 0, 12);
}

__global__ __launch_bounds__(512, 2) void k_p1(const u16* __restrict__ x, const u16* __restrict__ wqkv,
                                               const u16* __restrict__ wg, const u16* __restrict__ wu,
                                               char* __restrict__ ws) {
  extern __shared__ __attribute__((aligned(16))) char smem[];
  lptr sm = (lptr)smem;
  const int e = blockIdx.x & 7, idx = blockIdx.x >> 3;
  const int p = e >> 1, half = e & 1;
  if (idx < 12) {
    int rt = half * 4 + idx / 3, cg = 3 * p + idx % 3;
    p1_tile<true>(x, wqkv, wg, wu, ws, rt, cg, sm);
  } else {
    int k = idx - 12;
    int rt, c3;
    if (half == 0) { rt = k / 3; c3 = k % 3; }
    else { if (k == 0) { rt = 6; c3 = 2; } else { rt = 7 + (k - 1) / 3; c3 = (k - 1) % 3; } }
    if (rt > 12) return;
    p1_tile<false>(x, wqkv, wg, wu, ws, rt, 3 * p + c3, sm);
  }
}

constexpr int P2_PSTRIDE = 3072 + 32;
constexpr int P2_LDS = 21 * P2_PSTRIDE + 64;

DI void p2_block(char* __restrict__ ws, int pi, int hp, lptr sm) {
  const int tid = threadIdx.x, lane = tid & 63;
  const int wid = __builtin_amdgcn_readfirstlane(tid >> 6);
  STAMP(ws, 1, 0);
  {
    const int kp_min = pi >= 2 ? 0 : 2 - pi;
    for (int pc = wid; pc < 60; pc += 8) {
      int pl = pc / 3, kp = pc - pl * 3;
      if (kp >= kp_min) {
        int hh = pl / 10, w10 = pl - hh * 10;
        int which = 1 + (w10 >= 5), quad = w10 - (w10 >= 5 ? 5 : 0);
        int head = hp * 2 + hh;
        const char* src = ws + WS_QKV + (size_t)((which * 32 + head) * 5 + quad) * QKV_PLANE +
                          (size_t)((pi - 2 + kp) * 128) * 8 + lane * 16;
        glds16(src, sm + pl * P2_PSTRIDE + kp * 1024);
      }
    }
    if (tid < 384) *(LAS u32x2*)(sm + 20 * P2_PSTRIDE + tid * 8) = u32x2{0x00003f80u, 0u};
  }
  const int qt = wid & 3, hh = wid >> 2;
  const int head = hp * 2 + hh;
  const int q0 = pi * 128 + qt * 32;
  const int c = lane & 31, h = lane >> 5;
  bf16x8 qf0, qf1;
  {
    const char* qb = ws + WS_QKV + (size_t)(head * 5) * QKV_PLANE + (size_t)(q0 + c) * 8;
    u32x2 a = *(const u32x2*)(qb + (size_t)(2 * h) * QKV_PLANE);
    u32x2 b = *(const u32x2*)(qb + (size_t)(2 * h + 1) * QKV_PLANE);
    u32x2 d = *(const u32x2*)(qb + (size_t)4 * QKV_PLANE);
    u32x4 v0 = {a[0], a[1], b[0], b[1]};
    u32x4 v1 = {h ? 0u : d[0], h ? 0u : d[1], 0u, 0u};
    qf0 = __builtin_bit_cast(bf16x8, v0);
    qf1 = __builtin_bit_cast(bf16x8, v1);
  }
  WAITV(0);
  BARRIER();
  STAMP(ws, 1, 1);

  const int kt_min = q0 >= 256 ? 0 : (256 - q0) >> 5;
  lptr kb = sm + (hh * 10) * P2_PSTRIDE + (qt * 32 + c) * 8;
  f32x16 S[9];
#pragma unroll
  for (int kt = 0; kt < 9; ++kt) {
    if (kt >= kt_min) {
      u32x2 a = *(LAS u32x2*)(kb + (2 * h) * P2_PSTRIDE + kt * 256);
      u32x2 b = *(LAS u32x2*)(kb + (2 * h + 1) * P2_PSTRIDE + kt * 256);
      u32x2 d = *(LAS u32x2*)(kb + 4 * P2_PSTRIDE + kt * 256);
      u32x4 v0 = {a[0], a[1], b[0], b[1]};
      u32x4 v1 = {d[0], d[1], 0u, 0u};
      f32x16 z;
#pragma unroll
      for (int e = 0; e < 16; ++e) z[e] = 0.f;
      z = __builtin_amdgcn_mfma_f32_32x32x16_bf16(__builtin_bit_cast(bf16x8, v0), qf0, z, 0, 0, 0);
      z = __builtin_amdgcn_mfma_f32_32x32x16_bf16(__builtin_bit_cast(bf16x8, v1), qf1, z, 0, 0, 0);
      S[kt] = z;
    } else {
#pragma unroll
      for (int e = 0; e < 16; ++e) S[kt][e] = -INFINITY;
    }
  }
#pragma unroll
  for (int e = 0; e < 16; ++e) {
    int rr = (e & 3) + 8 * (e >> 2) + 4 * h;
    if (!(rr > c)) S[0][e] = -INFINITY;
    if (!(rr <= c)) S[8][e] = -INFINITY;
  }
  float mx = S[8][0];
#pragma unroll
  for (int kt = 0; kt < 9; ++kt)
#pragma unroll
    for (int e = 0; e < 16; ++e) mx = fmaxf(mx, S[kt][e]);
  mx = fmaxf(mx, __shfl_xor(mx, 32));
  STAMP(ws, 1, 2);
  const float C2 = 0.22360679774997896f * 1.4426950408889634f;
  const float nm = -mx * C2;

  f32x16 O;
#pragma unroll
  for (int e = 0; e < 16; ++e) O[e] = 0.f;
  {
    const int blk = (lane >> 4) & 1, q = (lane & 15) >> 2, pp = lane & 3;
    int pl = blk ? (pp == 0 ? hh * 10 + 9 : 20) : hh * 10 + 5 + pp;
    lptr vb = sm + pl * P2_PSTRIDE + (qt * 32 + 4 * h + q) * 8;
#pragma unroll
    for (int kt = 0; kt < 9; ++kt) {
      if (kt >= kt_min) {
#pragma unroll
        for (int s = 0; s < 2; ++s) {
          u32x4 pk;
#pragma unroll
          for (int e = 0; e < 4; ++e) {
            float p0 = __builtin_amdgcn_exp2f(__builtin_fmaf(S[kt][8 * s + 2 * e], C2, nm));
            float p1 = __builtin_amdgcn_exp2f(__builtin_fmaf(S[kt][8 * s + 2 * e + 1], C2, nm));
            pk[e] = pack2(p0, p1);
          }
          s16x4 lo = lds_tr(vb + kt * 256 + s * 128);
          s16x4 hi = lds_tr(vb + kt * 256 + s * 128 + 64);
          O = __builtin_amdgcn_mfma_f32_32x32x16_bf16(cat_tr(lo, hi), __builtin_bit_cast(bf16x8, pk), O, 0, 0, 0);
        }
      }
    }
  }
  STAMP(ws, 1, 3);
  float l = __shfl(O[8], c + 32);
  float inv = 1.0f / l;
  {
    const int m = q0 + c;
    char* ob = ws + WS_A2 + (size_t)(m >> 4) * KS2 * 1024 + (m & 15) * 16;
#pragma unroll
    for (int grp = 0; grp < 3; ++grp) {
      if (grp < 2 || h == 0) {
        int kidx = head * 20 + 8 * grp + 4 * h;
        u32x2 o2 = {pack2(O[4 * grp] * inv, O[4 * grp + 1] * inv), pack2(O[4 * grp + 2] * inv, O[4 * grp + 3] * inv)};
        *(u32x2*)(ob + (kidx >> 5) * 1024 + ((kidx >> 3) & 3) * 256 + ((kidx >> 2) & 1) * 8) = o2;
      }
    }
  }
  STAMP(ws, 1, 4);
}

__global__ __launch_bounds__(512, 2) void k_p2(char* __restrict__ ws) {
  extern __shared__ __attribute__((aligned(16))) char smem[];
  const int e = blockIdx.x & 7, idx = blockIdx.x >> 3;
  p2_block(ws, idx >> 1, e * 2 + (idx & 1), (lptr)smem);
}

constexpr int P3_STAGE = 40 * 1024;
constexpr int P3_LDS = 3 * P3_STAGE;

DI void p3_tile(const u16* __restrict__ x, const u16* __restrict__ wo, const u16* __restrict__ wd,
                const char* __restrict__ ws, u16* __restrict__ out, int rt, int ct, lptr sm) {
  const int tid = threadIdx.x, lane = tid & 63;
  const int wid = __builtin_amdgcn_readfirstlane(tid >> 6);
  const int kh = wid >> 2, mh = (wid >> 1) & 1, nh = wid & 1;
  unsigned aoff[3], boff[2];
#pragma unroll
  for (int i = 0; i < 3; ++i) {
    int p = wid + 8 * i;
    int mt = p >> 2, ks = p & 3;
    int gmt = rt * 6 + mt; gmt = gmt > 127 ? 127 : gmt;
    aoff[i] = (unsigned)((gmt * KS2 + ks) * 1024 + lane * 16);
  }
#pragma unroll
  for (int i = 0; i < 2; ++i) {
    int kb = wid + 8 * i;
    int r = lane >> 3, pc = lane & 7;
    int f = (((r >> 1) & 1) << 1) | ((kb & 1) << 2);
    int c = pc ^ f;
    boff[i] = (unsigned)((kb * 8 + r) * DM + ct * 64 + c * 8) * 2u;
  }
  auto issue = [&](int st, int slot) {
    const char* ab = ws + WS_A2 + (st < 12 ? (20 + 4 * st) : 4 * (st - 12)) * 1024;
    const char* bb = st < 12 ? (const char*)wd + (size_t)st * 128 * DM * 2 : (const char*)wo + (size_t)(st - 12) * 128 * DM * 2;
#pragma unroll
    for (int i = 0; i < 3; ++i) glds16(ab + aoff[i], sm + slot * P3_STAGE + (wid + 8 * i) * 1024);
#pragma unroll
    for (int i = 0; i < 2; ++i) glds16(bb + boff[i], sm + slot * P3_STAGE + (24 + wid + 8 * i) * 1024);
  };
  const int r = lane & 15, g = lane >> 4, q = (lane & 15) >> 2, pp = lane & 3;
  const unsigned xa = (unsigned)((mh * 3 * 4 + kh * 2) * 1024 + lane * 16);
  unsigned wa[2];
#pragma unroll
  for (int j = 0; j < 2; ++j) {
    int nt = 2 * nh + j;
    int f2 = (q >> 1) | ((g & 1) << 1);
    wa[j] = (unsigned)(24 * 1024 + (kh * 8 + g) * 1024 + q * 128 + ((nt ^ f2) * 32) + pp * 8);
  }
  f32x4 acc[3][2];
#pragma unroll
  for (int i = 0; i < 3; ++i)
#pragma unroll
    for (int j = 0; j < 2; ++j) acc[i][j] = f32x4{0.f, 0.f, 0.f, 0.f};

  STAMP((char*)ws, 2, 0);
  issue(0, 0);
  issue(1, 1);
#pragma unroll
  for (int t = 0; t < 17; ++t) {
    if (t < 16) WAITV(5); else WAITV(0);
    BARRIER();
    if (t == 0 || t == 4 || t == 8 || t == 12 || t == 16) STAMP((char*)ws, 2, 1 + t / 4);
    if (t + 2 < 17) issue(t + 2, (t + 2) % 3);
    lptr sb = sm + (t % 3) * P3_STAGE;
#pragma unroll
    for (int s = 0; s < 2; ++s) {
      bf16x8 xf[3], wf[2];
#pragma unroll
      for (int j = 0; j < 2; ++j) {
        s16x4 lo = lds_tr(sb + wa[j] + s * 4096);
        s16x4 hi = lds_tr(sb + wa[j] + s * 4096 + 512);
        wf[j] = cat_tr(lo, hi);
      }
#pragma unroll
      for (int i = 0; i < 3; ++i) xf[i] = lds_b128(sb + xa + i * 4096 + s * 1024);
#pragma unroll
      for (int i = 0; i < 3; ++i)
#pragma unroll
        for (int j = 0; j < 2; ++j)
          acc[i][j] = __builtin_amdgcn_mfma_f32_16x16x32_bf16(wf[j], xf[i], acc[i][j], 0, 0, 0);
    }
  }
  BARRIER();
  STAMP((char*)ws, 2, 6);
  constexpr int RS = 68;
  {
    LAS float* red = (LAS float*)sm + kh * 96 * RS;
#pragma unroll
    for (int i = 0; i < 3; ++i)
#pragma unroll
      for (int j = 0; j < 2; ++j)
        *(LAS f32x4*)(red + ((mh * 3 + i) * 16 + r) * RS + (2 * nh + j) * 16 + 4 * g) = acc[i][j];
  }
  BARRIER();
  for (int it = tid; it < 96 * 8; it += 512) {
    int ml = it >> 3, c8 = it & 7;
    int m = rt * 96 + ml;
    if (m < SEQ) {
      LAS float* r0 = (LAS float*)sm + ml * RS + c8 * 8;
      LAS float* r1 = r0 + 96 * RS;
      f32x4 a0 = *(LAS f32x4*)r0, a1 = *(LAS f32x4*)(r0 + 4);
      f32x4 b0 = *(LAS f32x4*)r1, b1 = *(LAS f32x4*)(r1 + 4);
      u32x4 xv = *(const u32x4*)(x + (size_t)m * DM + ct * 64 + c8 * 8);
      float v[8];
#pragma unroll
      for (int e = 0; e < 4; ++e) { v[e] = a0[e] + b0[e]; v[4 + e] = a1[e] + b1[e]; }
      u32x4 o;
#pragma unroll
      for (int e = 0; e < 4; ++e) {
        float lo = __uint_as_float(xv[e] << 16), hi = __uint_as_float(xv[e] & 0xffff0000u);
        o[e] = pack2(lo + v[2 * e], hi + v[2 * e + 1]);
      }
      *(u32x4*)(out + (size_t)m * DM + ct * 64 + c8 * 8) = o;
    }
  }
  STAMP((char*)ws, 2, 7);
}

__global__ __launch_bounds__(512, 2) void k_p3(const u16* __restrict__ x, const u16* __restrict__ wo,
                                               const u16* __restrict__ wd, const char* __restrict__ ws,
                                               u16* __restrict__ out) {
  extern __shared__ __attribute__((aligned(16))) char smem[];
  const int e = blockIdx.x & 7, idx = blockIdx.x >> 3;
  const int qd = e >> 1;
  const int start = (qd * 110 + 3) / 4;
  const int end = ((qd + 1) * 110 + 3) / 4;
  const int li = start + idx;
  if (li >= end) return;
  p3_tile(x, wo, wd, ws, out, li / 5, (e & 1) * 5 + li % 5, (lptr)smem);
  if (blockIdx.x == 0) {
    __syncthreads();
    if (threadIdx.x == 0) {
      unsigned long long id = hw_dispatch_id();
      unsigned long long* w = (unsigned long long*)(ws + (128u << 20));
      unsigned long long old = __hip_atomic_load(w, __ATOMIC_RELAXED, __HIP_MEMORY_SCOPE_AGENT);
      unsigned long long tag = id * 0x9E3779B97F4A7C15ull + 0x1234567;
      if (old == tag) out[0] = 0x7fc0;
      __hip_atomic_store(w, tag, __ATOMIC_RELAXED, __HIP_MEMORY_SCOPE_AGENT);
    }
  }
}

extern "C" void kernel_launch(void* const* d_in, const int* in_sizes, int n_in,
                              void* d_out, int out_size, void* d_ws, size_t ws_size,
                              hipStream_t stream) {
  const u16* x = (const u16*)d_in[0];
  const u16* wqkv = (const u16*)d_in[1];
  const u16* wo = (const u16*)d_in[2];
  const u16* wg = (const u16*)d_in[4];
  const u16* wu = (const u16*)d_in[5];
  const u16* wd = (const u16*)d_in[6];
  char* ws = (char*)d_ws;
  (void)hipFuncSetAttribute((const void*)k_p1, hipFuncAttributeMaxDynamicSharedMemorySize, P1_LDS);
  (void)hipFuncSetAttribute((const void*)k_p2, hipFuncAttributeMaxDynamicSharedMemorySize, P2_LDS);
  (void)hipFuncSetAttribute((const void*)k_p3, hipFuncAttributeMaxDynamicSharedMemorySize, P3_LDS);
  k_p1<<<256, 512, P1_LDS, stream>>>(x, wqkv, wg, wu, ws);
  k_p2<<<256, 512, P2_LDS, stream>>>(ws);
  k_p3<<<224, 512, P3_LDS, stream>>>(x, wo, wd, ws, (u16*)d_out);
}
```

```cpp
#include <hip/hip_runtime.h>
#include <hip/hip_bf16.h>

typedef __attribute__((ext_vector_type(8))) __bf16 bf16x8;
typedef __attribute__((ext_vector_type(2))) __bf16 bf16x2;
typedef __attribute__((ext_vector_type(4))) short s16x4;
typedef __attribute__((ext_vector_type(4))) float f32x4;
typedef __attribute__((ext_vector_type(2))) float f32x2;
typedef __attribute__((ext_vector_type(16))) float f32x16;
typedef __attribute__((ext_vector_type(4))) unsigned u32x4;
typedef __attribute__((ext_vector_type(2))) unsigned u32x2;
#define LAS __attribute__((address_space(3)))
typedef LAS char* lptr;
typedef unsigned short u16;
typedef unsigned long long u64;

#define DI __device__ __forceinline__
extern "C" __device__ u64 hw_dispatch_id() __asm("llvm.amdgcn.dispatch.id");

constexpr int SEQ = 2048, DM = 640, FF = 1536;
constexpr int KS2 = 68;
constexpr size_t QKV_PLANE = (size_t)SEQ * 8;
constexpr size_t WS_QKV = 0;
constexpr size_t WS_A2 = WS_QKV + 3 * 32 * 5 * QKV_PLANE;
constexpr size_t WS_ROWST = WS_A2 + (size_t)128 * KS2 * 1024;
constexpr size_t WS_COLST = WS_ROWST + 2048 * 16;
constexpr size_t WS_FLAGQ = WS_COLST + 3 * 2496 * 16;
constexpr size_t WS_FLAGG = WS_FLAGQ + 8 * 128;
constexpr size_t WS_FLAGO = WS_FLAGG + 13 * 128;

DI void glds16(const void* g, lptr l) {
  __builtin_amdgcn_global_load_lds((const __attribute__((address_space(1))) unsigned*)g, (LAS unsigned*)l, 16, 0, 0);
}
DI void glds16_dev(const void* g, lptr l) {
  __builtin_amdgcn_global_load_lds((const __attribute__((address_space(1))) unsigned*)g, (LAS unsigned*)l, 16, 0, 16);
}
DI float dot2(unsigned a, unsigned b, float c) {
  return __builtin_amdgcn_fdot2_f32_bf16(__builtin_bit_cast(bf16x2, a), __builtin_bit_cast(bf16x2, b), c, false);
}
DI unsigned pack2(float a, float b) {
  bf16x2 v; v[0] = (__bf16)a; v[1] = (__bf16)b;
  return __builtin_bit_cast(unsigned, v);
}
#define STAMP(ws, i) do {} while (0)
#define WAITV(n) asm volatile("s_waitcnt vmcnt(" #n ")" ::: "memory")
#define BARRIER() do { asm volatile("" ::: "memory"); __builtin_amdgcn_s_waitcnt(0xC07F); __builtin_amdgcn_s_barrier(); asm volatile("" ::: "memory"); } while (0)
DI u32x4 lds_u128(lptr p) { return *(LAS u32x4*)p; }
DI u32x2 lds_tru(lptr p) { return __builtin_bit_cast(u32x2, __builtin_amdgcn_ds_read_tr16_b64_v4i16((LAS s16x4*)p)); }
DI bf16x8 cat_u(u32x2 lo, u32x2 hi) { u32x4 v = {lo[0], lo[1], hi[0], hi[1]}; return __builtin_bit_cast(bf16x8, v); }
DI lptr lp(unsigned a) { return (lptr)(size_t)a; }

DI void st8_dev(void* p, u32x2 v) {
  __hip_atomic_store((u64*)p, ((u64)v[1] << 32) | v[0], __ATOMIC_RELAXED, __HIP_MEMORY_SCOPE_AGENT);
}
DI void st16_dev(void* p, u32x4 v) {
  asm volatile("global_store_dwordx4 %0, %1, off sc1\n\ts_nop 1" :: "v"(p), "v"(v) : "memory");
}
DI u32x4 ld16_dev(const void* p) {
  u32x4 v;
  asm volatile("global_load_dwordx4 %0, %1, off sc1\n\ts_waitcnt vmcnt(0)" : "=&v"(v) : "v"(p) : "memory");
  return v;
}
DI u64 ld8_dev(const void* p) { return __hip_atomic_load((const u64*)p, __ATOMIC_RELAXED, __HIP_MEMORY_SCOPE_AGENT); }
constexpr unsigned SPIN_MAX = 1u << 22;
DI void wave_wait_flags(const char* p, bool act, u64 tag) {
  for (unsigned spins = 0; spins < SPIN_MAX; ++spins) {
    bool ok = true;
    if (act) ok = (ld8_dev(p) == tag);
    if (__all(ok)) break;
    __builtin_amdgcn_s_sleep(1);
  }
  asm volatile("" ::: "memory");
}

template <int N> DI float add_ror(float v) {
  return v + __builtin_bit_cast(float, __builtin_amdgcn_update_dpp(0, __builtin_bit_cast(int, v), 0x120 + N, 0xf, 0xf, false));
}
DI float add_x16(float v) {
  auto r = __builtin_amdgcn_permlane16_swap(__float_as_uint(v), __float_as_uint(v), false, false);
  return __uint_as_float(r[0]) + __uint_as_float(r[1]);
}
DI float add_x32(float v) {
  auto r = __builtin_amdgcn_permlane32_swap(__float_as_uint(v), __float_as_uint(v), false, false);
  return __uint_as_float(r[0]) + __uint_as_float(r[1]);
}
DI float max_x32(float v) {
  auto r = __builtin_amdgcn_permlane32_swap(__float_as_uint(v), __float_as_uint(v), false, false);
  return fmaxf(__uint_as_float(r[0]), __uint_as_float(r[1]));
}

constexpr int P1_STAGE = 52 * 1024;
constexpr int P1_STATS = 3 * P1_STAGE;
constexpr int LDS_TOTAL = 160 * 1024;

struct StatLoads { u32x4 r0, r1, c0, c1, c2, c3; };

DI void p0_issue(const u16* x, const u16* wqkv, const u16* wg, const u16* wu, StatLoads& L) {
  const int lane = threadIdx.x & 63, wid = threadIdx.x >> 6, b = blockIdx.x;
  const char* rp = (const char*)(x + (size_t)(8 * b + wid) * DM);
  const char* a0 = rp + lane * 16;
  const char* a1 = rp + 1024 + (lane & 15) * 16;
  int T = b < 234 ? b : 233;
  int u = T / 3, part = T - u * 3;
  const char* wb; int rs;
  if (u < 30) { wb = (const char*)(wqkv + u * 64); rs = 3 * DM * 2; }
  else if (u < 54) { wb = (const char*)(wg + (u - 30) * 64); rs = FF * 2; }
  else { wb = (const char*)(wu + (u - 54) * 64); rs = FF * 2; }
  const char* ca[4];
#pragma unroll
  for (int i = 0; i < 4; ++i) {
    int kr = part * 214 + (wid + 8 * i) * 8 + (lane >> 3);
    kr = kr > DM - 1 ? DM - 1 : kr;
    ca[i] = wb + (size_t)kr * rs + (lane & 7) * 16;
  }
  L.r0 = *(const u32x4*)a0; L.r1 = *(const u32x4*)a1;
  L.c0 = *(const u32x4*)ca[0]; L.c1 = *(const u32x4*)ca[1]; L.c2 = *(const u32x4*)ca[2]; L.c3 = *(const u32x4*)ca[3];
}
DI void p0_compute(StatLoads& L, char* ws, u64 tag, lptr red) {
  const int lane = threadIdx.x & 63, wid = threadIdx.x >> 6, b = blockIdx.x;
  const unsigned ONES = 0x3f803f80u;
  float s = 0.f, q = 0.f;
#pragma unroll
  for (int e = 0; e < 4; ++e) {
    unsigned a = L.r0[e], c = lane < 16 ? L.r1[e] : 0u;
    s = dot2(a, ONES, s); q = dot2(a, a, q);
    s = dot2(c, ONES, s); q = dot2(c, c, q);
  }
  s = add_ror<1>(s); q = add_ror<1>(q); s = add_ror<2>(s); q = add_ror<2>(q);
  s = add_ror<4>(s); q = add_ror<4>(q); s = add_ror<8>(s); q = add_ror<8>(q);
  s = add_x16(s); q = add_x16(q); s = add_x32(s); q = add_x32(q);
  {
    const float inv = 1.0f / DM;
    float m = s * inv;
    float v = fmaxf(q * inv - m * m, 0.f);
    float r = rsqrtf(v + 1e-5f);
    if (lane == 0) {
      u32x4 g = {__float_as_uint(m), __float_as_uint(r), (unsigned)tag, (unsigned)(tag >> 32)};
      st16_dev(ws + WS_ROWST + (size_t)(8 * b + wid) * 16, g);
    }
  }
  int T = b < 234 ? b : 233;
  int part = T % 3;
  int nrows = part < 2 ? 214 : 212;
  float cs[8];
#pragma unroll
  for (int e = 0; e < 8; ++e) cs[e] = 0.f;
#pragma unroll
  for (int i = 0; i < 4; ++i) {
    u32x4 v = i == 0 ? L.c0 : i == 1 ? L.c1 : i == 2 ? L.c2 : L.c3;
    bool ok = (wid + 8 * i) * 8 + (lane >> 3) < nrows;
#pragma unroll
    for (int e = 0; e < 4; ++e) {
      unsigned w = ok ? v[e] : 0u;
      cs[2 * e] += __uint_as_float(w << 16);
      cs[2 * e + 1] += __uint_as_float(w & 0xffff0000u);
    }
  }
#pragma unroll
  for (int e = 0; e < 8; ++e) cs[e] = add_x32(add_x16(add_ror<8>(cs[e])));
  if (lane < 8) {
    *(LAS f32x4*)(red + wid * 256 + lane * 32) = f32x4{cs[0], cs[1], cs[2], cs[3]};
    *(LAS f32x4*)(red + wid * 256 + lane * 32 + 16) = f32x4{cs[4], cs[5], cs[6], cs[7]};
  }
}
DI void p0_publish(char* ws, u64 tag, lptr red) {
  const int tid = threadIdx.x, b = blockIdx.x;
  if (tid < 32 && b < 234) {
    float c0 = 0.f, c1 = 0.f;
#pragma unroll
    for (int w = 0; w < 8; ++w) { f32x2 v = *(LAS f32x2*)(red + w * 256 + tid * 8); c0 += v[0]; c1 += v[1]; }
    int u = b / 3, part = b - u * 3;
    u32x4 g = {__float_as_uint(c0), __float_as_uint(c1), (unsigned)tag, (unsigned)(tag >> 32)};
    st16_dev(ws + WS_COLST + ((size_t)part * 2496 + u * 32 + tid) * 16, g);
  }
}

struct Work {
  int p1type;
  int p1rt, p1cg;
  int pi, hp;
  int p3rt, p3ct;
};
DI Work get_work() {
  Work w;
  const int e = blockIdx.x & 7, idx = blockIdx.x >> 3;
  const int p = e >> 1, half = e & 1;
  if (idx < 12) { w.p1type = 0; w.p1rt = half * 4 + idx / 3; w.p1cg = 3 * p + idx % 3; }
  else {
    int k = idx - 12, rt, c3;
    if (half == 0) { rt = k / 3; c3 = k % 3; }
    else { if (k == 0) { rt = 6; c3 = 2; } else { rt = 7 + (k - 1) / 3; c3 = (k - 1) % 3; } }
    w.p1type = rt <= 12 ? 1 : 2; w.p1rt = rt; w.p1cg = 3 * p + c3;
  }
  w.pi = idx >> 1; w.hp = e * 2 + (idx & 1);
  const int qd = e >> 1;
  const int start = (qd * 110 + 3) / 4, end = ((qd + 1) * 110 + 3) / 4;
  const int li = start + idx;
  w.p3rt = li < end ? li / 5 : -1; w.p3ct = (e & 1) * 5 + li % 5;
  return w;
}

constexpr int P2_PSTRIDE = 3072 + 32;
constexpr int P2_QOFF = 21 * P2_PSTRIDE;
constexpr int P3_STAGE = 40 * 1024;

constexpr int P2_GO = P2_QOFF + 10 * 1024;
DI void p2_stage(const char* ws, int pi, int hp, lptr sm, int w12) {
  const int lane = threadIdx.x & 63;
  const int kp_min = pi >= 2 ? 0 : 2 - pi;
#pragma unroll 1
  for (int pc = w12; pc < 70; pc += 12) {
    if (pc < 60) {
      int pl = pc / 3, kp = pc - pl * 3;
      if (kp >= kp_min) {
        int hh = pl / 10, w10 = pl - hh * 10;
        int which2 = 1 + (w10 >= 5), quad = w10 - (w10 >= 5 ? 5 : 0);
        const char* src = ws + WS_QKV + (size_t)((which2 * 32 + hp * 2 + hh) * 5 + quad) * QKV_PLANE +
                          (size_t)((pi - 2 + kp) * 128) * 8 + lane * 16;
        glds16_dev(src, sm + pl * P2_PSTRIDE + kp * 1024);
      }
    } else {
      int pl = pc - 60;
      const char* src = ws + WS_QKV + (size_t)(hp * 10 + pl) * QKV_PLANE + (size_t)(pi * 128) * 8 + lane * 16;
      glds16_dev(src, sm + P2_QOFF + pl * 1024);
    }
  }
  WAITV(0);
}

template <bool ISQ>
DI void p1_load(const u16* __restrict__ x, const u16* __restrict__ wqkv, const u16* __restrict__ wg,
                const u16* __restrict__ wu, char* __restrict__ ws, int rt, int cg, lptr sm, u64 tag, int lw) {
  constexpr int BM = ISQ ? 256 : 160, BN = ISQ ? 160 : 256;
  constexpr int APC = BM / 8, NA = APC / 4;
  const int lane = threadIdx.x & 63;
  const int row0 = rt * BM;
  unsigned poff[13];
#pragma unroll
  for (int i = 0; i < 13; ++i) {
    int p = lw + 4 * i;
    if (i < NA) {
      int rl = 8 * p + (lane >> 3), pc = lane & 7;
      int c = pc ^ ((rl >> 1) & 7);
      int gr = row0 + rl; gr = gr > SEQ - 1 ? SEQ - 1 : gr;
      poff[i] = (unsigned)(gr * DM + c * 8) * 2u;
    } else {
      int pb = p - APC;
      if (ISQ) {
        int ci = pb * 64 + lane;
        int kr = ci / 20, pc = ci - kr * 20;
        int c = pc ^ (((kr >> 3) & 1) << 1);
        poff[i] = (unsigned)(kr * (3 * DM) + cg * 160 + c * 8) * 2u;
      } else {
        int u = pb >> 3, kb = pb & 7;
        int r = lane >> 3, pc = lane & 7;
        int f = (((r >> 1) & 1) << 1) | ((kb & 1) << 2);
        int c = pc ^ f;
        poff[i] = (unsigned)((kb * 8 + r) * FF + cg * 128 + (u & 1) * 64 + c * 8) * 2u;
      }
    }
  }
  const char* xb = (const char*)x;
  const char* w0 = (const char*)(ISQ ? wqkv : wg);
  const char* w1 = (const char*)(ISQ ? wqkv : wu);
  auto issue = [&](int so) {
#pragma unroll
    for (int i = 0; i < 13; ++i) {
      const char* b = i < NA ? xb : ((i - NA) < 4 ? w0 : w1);
      glds16(b + poff[i], sm + so + (lw + 4 * i) * 1024);
    }
    xb += 128;
    w0 += ISQ ? 64 * 3 * DM * 2 : 64 * FF * 2;
    w1 += ISQ ? 64 * 3 * DM * 2 : 64 * FF * 2;
  };
  issue(0);
  issue(P1_STAGE);
  issue(2 * P1_STAGE);
  WAITV(26);
  BARRIER();
  int so = 0;
#pragma unroll 1
  for (int t = 0; t < 8; ++t) {
    WAITV(13);
    BARRIER();
    if (t < 7) issue(so);
    so = (so == 2 * P1_STAGE) ? 0 : so + P1_STAGE;
  }
  {
    LAS f32x2* rowstat = (LAS f32x2*)(sm + P1_STATS);
    LAS float* colsum = (LAS float*)(sm + P1_STATS + 2048);
    const int lt = lw * 64 + lane;
    int gr = row0 + (lt < BM ? lt : 0); gr = gr > SEQ - 1 ? SEQ - 1 : gr;
    const int lc = lt < BN / 2 ? lt : 0;
    const int gc = ISQ ? cg * 80 + lc : (lc < 64 ? 960 + cg * 64 + lc : 1728 + cg * 64 + (lc - 64));
    const char* g0 = ws + WS_ROWST + (size_t)gr * 16;
    const char* g1 = ws + WS_COLST + (size_t)gc * 16;
    const char* g2 = g1 + 2496 * 16;
    const char* g3 = g2 + 2496 * 16;
    u32x4 v0, v1, v2, v3;
    for (unsigned spins = 0; spins < SPIN_MAX; ++spins) {
      asm volatile("global_load_dwordx4 %0, %4, off sc1\n\tglobal_load_dwordx4 %1, %5, off sc1\n\t"
                   "global_load_dwordx4 %2, %6, off sc1\n\tglobal_load_dwordx4 %3, %7, off sc1\n\ts_waitcnt vmcnt(0)"
                   : "=&v"(v0), "=&v"(v1), "=&v"(v2), "=&v"(v3) : "v"(g0), "v"(g1), "v"(g2), "v"(g3) : "memory");
      const unsigned tl = (unsigned)tag, th = (unsigned)(tag >> 32);
      bool ok = v0[2] == tl && v0[3] == th && v1[2] == tl && v1[3] == th && v2[2] == tl && v2[3] == th && v3[2] == tl && v3[3] == th;
      if (__all(ok)) break;
      __builtin_amdgcn_s_sleep(1);
    }
    if (lt < BM) rowstat[lt] = f32x2{__uint_as_float(v0[0]), __uint_as_float(v0[1])};
    if (lt < BN / 2)
      *(LAS f32x2*)(colsum + 2 * lt) = f32x2{__uint_as_float(v1[0]) + __uint_as_float(v2[0]) + __uint_as_float(v3[0]),
                                             __uint_as_float(v1[1]) + __uint_as_float(v2[1]) + __uint_as_float(v3[1])};
  }
  BARRIER();
  BARRIER();
}

struct P3Load {
  unsigned aoff[6], boff[4];
};
DI void p3_load_setup(P3Load& L, int rt, int ct, int lw) {
  const int lane = threadIdx.x & 63;
#pragma unroll
  for (int i = 0; i < 6; ++i) {
    int p = lw + 4 * i;
    int gmt = rt * 6 + (p >> 2); gmt = gmt > 127 ? 127 : gmt;
    L.aoff[i] = (unsigned)((gmt * KS2 + (p & 3)) * 1024 + lane * 16);
  }
#pragma unroll
  for (int i = 0; i < 4; ++i) {
    int kb = lw + 4 * i;
    int r = lane >> 3, pc = lane & 7;
    int f = (((r >> 1) & 1) << 1) | ((kb & 1) << 2);
    int c = pc ^ f;
    L.boff[i] = (unsigned)((kb * 8 + r) * DM + ct * 64 + c * 8) * 2u;
  }
}
DI void p3_issue_a(const P3Load& L, const char* ws, int st, lptr sm, int lw) {
  const char* ab = ws + WS_A2 + (st < 12 ? (20 + 4 * st) : 4 * (st - 12)) * 1024;
  lptr d = sm + ((st + 2) & 3) * P3_STAGE;
#pragma unroll
  for (int i = 0; i < 6; ++i) glds16_dev(ab + L.aoff[i], d + (lw + 4 * i) * 1024);
}
DI void p3_issue_b(const P3Load& L, const u16* wo, const u16* wd, int st, lptr sm, int lw) {
  const char* bb = st < 12 ? (const char*)wd + (size_t)st * 128 * DM * 2 : (const char*)wo + (size_t)(st - 12) * 128 * DM * 2;
  lptr d = sm + ((st + 2) & 3) * P3_STAGE;
#pragma unroll
  for (int i = 0; i < 4; ++i) glds16(bb + L.boff[i], d + (24 + lw + 4 * i) * 1024);
}

DI void loader_main(const u16* x, const u16* wqkv, const u16* wo, const u16* wg, const u16* wu, const u16* wd,
                    char* ws, lptr sm, u64 tag, int lw) {
  const Work W = get_work();
  const int lane = threadIdx.x & 63;
  if (W.p1type == 0) p1_load<true>(x, wqkv, wg, wu, ws, W.p1rt, W.p1cg, sm, tag, lw);
  else if (W.p1type == 1) p1_load<false>(x, wqkv, wg, wu, ws, W.p1rt, W.p1cg, sm, tag, lw);
  else BARRIER();
  const bool has3 = W.p3rt >= 0;
  P3Load L3;
  p3_load_setup(L3, has3 ? W.p3rt : 0, W.p3ct, lw);
  if (has3) p3_issue_b(L3, wo, wd, 0, sm, lw);
  BARRIER();
  if (has3) p3_issue_b(L3, wo, wd, 1, sm, lw);
  const int pi = W.pi, hp = W.hp;
  {
    int which = lane >> 1, rsel = lane & 1;
    int rtile = rsel == 0 ? (pi >> 1) : (pi >= 2 ? (pi - 2) >> 1 : 0);
    wave_wait_flags(ws + WS_FLAGQ + rtile * 128 + (which * 4 + (hp >> 2)) * 8, lane < 6, tag);
    if (lw == 0 && lane == 0) *(volatile LAS unsigned*)(sm + P2_GO) = 1u;
    p2_stage(ws, pi, hp, sm, 8 + lw);
  }
  BARRIER();
  const int r_lo = W.p3rt * 96, r_hi = (r_lo + 95 > SEQ - 1) ? SEQ - 1 : r_lo + 95;
  if (has3) {
    int sel = lane >> 4, cgx = lane & 15;
    int gt = sel == 0 ? r_lo / 160 : r_hi / 160;
    wave_wait_flags(ws + WS_FLAGG + gt * 128 + cgx * 8, lane < 32 && cgx < 12, tag);
    p3_issue_a(L3, ws, 0, sm, lw);
    p3_issue_a(L3, ws, 1, sm, lw);
  }
  BARRIER();
  if (!has3) return;
  p3_issue_a(L3, ws, 2, sm, lw);
#pragma unroll 1
  for (int t = 0; t < 17; ++t) {
    if (t < 15) WAITV(12); else if (t == 15) WAITV(6); else WAITV(0);
    BARRIER();
    if (t + 3 < 17) {
      if (t + 3 == 12) {
        int sel = lane >> 4, hpx = lane & 15;
        int pn = sel == 0 ? r_lo >> 7 : r_hi >> 7;
        wave_wait_flags(ws + WS_FLAGO + pn * 128 + hpx * 8, lane < 32, tag);
      }
      p3_issue_a(L3, ws, t + 3, sm, lw);
    }
  }
}

template <bool ISQ>
DI void p1_compute(const u16* __restrict__ x, const u16* __restrict__ wqkv, const u16* __restrict__ wg,
                   const u16* __restrict__ wu, char* __restrict__ ws, int rt, int cg, lptr sm, u64 tag) {
  constexpr int BM = ISQ ? 256 : 160;
  constexpr int MT = ISQ ? 4 : 5, NT = ISQ ? 5 : 4;
  constexpr int NWN = ISQ ? 2 : 4;
  constexpr int ABYTES = BM * 128;
  const int tid = threadIdx.x, lane = tid & 63;
  const int wid = __builtin_amdgcn_readfirstlane(tid >> 6);
  const int wm = wid / NWN, wn = wid % NWN;
  const int row0 = rt * BM;
  {
    StatLoads SL;
    p0_issue(x, wqkv, wg, wu, SL);
    p0_compute(SL, ws, tag, sm + P1_STATS);
  }
  const int r = lane & 15, g = lane >> 4, q = (lane & 15) >> 2, pp = lane & 3;
  unsigned xa0 = (unsigned)(size_t)sm + (unsigned)((wm * MT * 16 + r) * 128 + ((g ^ (r >> 1)) * 16));
  unsigned xa1 = xa0 ^ 64u;
  unsigned wa[2];
  if (ISQ) {
#pragma unroll
    for (int j = 0; j < 2; ++j) {
      int nt = 5 * wn + j;
      wa[j] = (unsigned)(size_t)sm + (unsigned)(ABYTES + (8 * g + q) * 320 + ((nt ^ (g & 1)) * 32) + pp * 8);
    }
  } else {
#pragma unroll
    for (int j = 0; j < 2; ++j) {
      int u = wn >> 1;
      int nt = 2 * (wn & 1) + j;
      int f2 = (q >> 1) | ((g & 1) << 1);
      wa[j] = (unsigned)(size_t)sm + (unsigned)(ABYTES + (u * 8 + g) * 1024 + q * 128 + ((nt ^ f2) * 32) + pp * 8);
    }
  }
  constexpr int W_T = ISQ ? 4 * 320 : 512;
  constexpr int W_S = ISQ ? 32 * 320 : 4096;
  constexpr int W_J = ISQ ? 64 : 16384;

  f32x4 acc[MT][NT];
#pragma unroll
  for (int i = 0; i < MT; ++i)
#pragma unroll
    for (int j = 0; j < NT; ++j) acc[i][j] = f32x4{0.f, 0.f, 0.f, 0.f};

  u32x4 xA[MT], xB[MT];
  u32x2 wAl[NT], wAh[NT], wBl[NT], wBh[NT];
  constexpr int NRD = 2 * NT + MT;
  auto rd = [&](int ri, u32x4* xn, u32x2* wl, u32x2* wh, unsigned xad, int s) {
    if (ri == 2) { xn[0] = lds_u128(lp(xad)); return; }
    int k = ri < 2 ? ri : ri - 1;
    if (k < 2 * NT) {
      int j = k >> 1;
      u32x2 v = lds_tru(lp(wa[j & 1]) + (j >> 1) * W_J + s * W_S + (k & 1) * W_T);
      if (k & 1) wh[j] = v; else wl[j] = v;
    } else {
      int i = k - 2 * NT + 1;
      xn[i] = lds_u128(lp(xad) + i * 2048);
    }
  };
  auto kstep = [&](const u32x4* xc, const u32x2* wcl, const u32x2* wch, u32x4* xn, u32x2* wnl, u32x2* wnh,
                   unsigned xad, int sn) {
#pragma unroll
    for (int k = 0; k < 20; ++k) {
      const int i = k / NT, j = k % NT;
      acc[i][j] = __builtin_amdgcn_mfma_f32_16x16x32_bf16(cat_u(wcl[j], wch[j]), __builtin_bit_cast(bf16x8, xc[i]), acc[i][j], 0, 0, 0);
      __builtin_amdgcn_sched_barrier(0);
      if (k < NRD) rd(k, xn, wnl, wnh, xad, sn);
      __builtin_amdgcn_sched_barrier(0);
    }
  };

  BARRIER();
  STAMP(ws, 1);
  p0_publish(ws, tag, sm + P1_STATS);
#pragma unroll
  for (int ri = 0; ri < NRD; ++ri) rd(ri, xA, wAl, wAh, xa0, 0);
  int so_cur = 0;
#pragma unroll 1
  for (int t = 0; t < 10; ++t) {
    const int so_n = (so_cur == 2 * P1_STAGE) ? 0 : so_cur + P1_STAGE;
    const int dlt = so_n - so_cur;
    kstep(xA, wAl, wAh, xB, wBl, wBh, xa1, 1);
    xa0 += dlt; xa1 += dlt; wa[0] += dlt; wa[1] += dlt;
    BARRIER();
    STAMP(ws, 2 + t);
    kstep(xB, wBl, wBh, xA, wAl, wAh, xa0, 0);
    so_cur = so_n;
  }
  STAMP(ws, 12);

  LAS f32x2* rowstat = (LAS f32x2*)(sm + P1_STATS);
  LAS float* colsum = (LAS float*)(sm + P1_STATS + 2048);
  f32x4 csv[NT];
#pragma unroll
  for (int j = 0; j < NT; ++j) {
    int nb = ISQ ? 5 * wn + j : (j >> 1) * 8 + 2 * wn + (j & 1);
    csv[j] = *(LAS f32x4*)(colsum + nb * 16 + 4 * g);
  }
#pragma unroll
  for (int i = 0; i < MT; ++i) {
    const int ml = wm * MT * 16 + i * 16 + r;
    const f32x2 st = rowstat[ml];
    const int m = row0 + ml;
    if (ISQ) {
      const int which = cg >> 2;
#pragma unroll
      for (int j = 0; j < NT; ++j) {
        int col = (5 * wn + j) * 16 + 4 * g;
        int hl = col / 20, quad = (col - hl * 20) >> 2;
        int head = (cg & 3) * 8 + hl;
        f32x4 y;
#pragma unroll
        for (int e = 0; e < 4; ++e) y[e] = st[1] * (acc[i][j][e] - st[0] * csv[j][e]);
        u32x2 o2 = {pack2(y[0], y[1]), pack2(y[2], y[3])};
        st8_dev(ws + WS_QKV + (size_t)((which * 32 + head) * 5 + quad) * QKV_PLANE + (size_t)m * 8, o2);
      }
    } else {
      if (m < SEQ) {
#pragma unroll
        for (int tp = 0; tp < 2; ++tp) {
          float hv[4];
#pragma unroll
          for (int e = 0; e < 4; ++e) {
            float gt = st[1] * (acc[i][tp][e] - st[0] * csv[tp][e]);
            float up = st[1] * (acc[i][tp + 2][e] - st[0] * csv[tp + 2][e]);
            float sg = __builtin_amdgcn_rcpf(1.0f + __expf(-gt));
            hv[e] = gt * sg * up;
          }
          u32x2 o2 = {pack2(hv[0], hv[1]), pack2(hv[2], hv[3])};
          int ks = 20 + 4 * cg + wn;
          int gg = 2 * tp + (g >> 1);
          st8_dev(ws + WS_A2 + ((size_t)(m >> 4) * KS2 + ks) * 1024 + (gg * 16 + r) * 16 + (g & 1) * 8, o2);
        }
      }
    }
  }
  if (tid == 0) *(volatile LAS unsigned*)(sm + P2_GO) = 0u;
  WAITV(0);
  BARRIER();
  if (tid == 0)
    __hip_atomic_store((u64*)(ws + (ISQ ? WS_FLAGQ : WS_FLAGG) + rt * 128 + cg * 8), tag, __ATOMIC_RELAXED, __HIP_MEMORY_SCOPE_AGENT);
  STAMP(ws, 14);
}

DI void p2_compute(char* __restrict__ ws, int pi, int hp, lptr sm, u64 tag) {
  const int tid = threadIdx.x, lane = tid & 63;
  const int wid = __builtin_amdgcn_readfirstlane(tid >> 6);
  if (tid < 384) *(LAS u32x2*)(sm + 20 * P2_PSTRIDE + tid * 8) = u32x2{0x00003f80u, 0u};
  for (unsigned spins = 0; spins < (SPIN_MAX << 6); ++spins) {
    if (*(volatile LAS unsigned*)(sm + P2_GO) != 0u) break;
    __builtin_amdgcn_s_sleep(2);
  }
  asm volatile("" ::: "memory");
  p2_stage(ws, pi, hp, sm, wid);
  BARRIER();
  STAMP(ws, 17);
  const int qt = wid & 3, hh = wid >> 2;
  const int head = hp * 2 + hh;
  const int q0 = pi * 128 + qt * 32;
  const int c = lane & 31, h = lane >> 5;
  bf16x8 qf0, qf1;
  {
    lptr qb = sm + P2_QOFF + hh * 5 * 1024 + (qt * 32 + c) * 8;
    u32x2 a = *(LAS u32x2*)(qb + (2 * h) * 1024);
    u32x2 b = *(LAS u32x2*)(qb + (2 * h + 1) * 1024);
    u32x2 d = *(LAS u32x2*)(qb + 4 * 1024);
    u32x4 v0 = {a[0], a[1], b[0], b[1]};
    u32x4 v1 = {h ? 0u : d[0], h ? 0u : d[1], 0u, 0u};
    qf0 = __builtin_bit_cast(bf16x8, v0);
    qf1 = __builtin_bit_cast(bf16x8, v1);
  }
  const int kt_min = q0 >= 256 ? 0 : (256 - q0) >> 5;
  lptr kb = sm + (hh * 10) * P2_PSTRIDE + (qt * 32 + c) * 8;
  lptr vb;
  {
    const int blk = (lane >> 4) & 1, q = (lane & 15) >> 2, pp = lane & 3;
    int pl = blk ? (pp == 0 ? hh * 10 + 9 : 20) : hh * 10 + 5 + pp;
    vb = sm + pl * P2_PSTRIDE + (qt * 32 + 4 * h + q) * 8;
  }
  const float C2 = 0.22360679774997896f * 1.4426950408889634f;
  f32x16 O;
#pragma unroll
  for (int e = 0; e < 16; ++e) O[e] = 0.f;
  float mrun = -INFINITY;
#pragma unroll
  for (int ch = 2; ch >= 0; --ch) {
    if (3 * ch + 2 >= kt_min) {
      f32x16 S[3];
#pragma unroll
      for (int u = 0; u < 3; ++u) {
        const int kt = 3 * ch + u;
        if (kt >= kt_min) {
          u32x2 a = *(LAS u32x2*)(kb + (2 * h) * P2_PSTRIDE + kt * 256);
          u32x2 b = *(LAS u32x2*)(kb + (2 * h + 1) * P2_PSTRIDE + kt * 256);
          u32x2 d = *(LAS u32x2*)(kb + 4 * P2_PSTRIDE + kt * 256);
          u32x4 v0 = {a[0], a[1], b[0], b[1]};
          u32x4 v1 = {d[0], d[1], 0u, 0u};
          f32x16 z;
#pragma unroll
          for (int e = 0; e < 16; ++e) z[e] = 0.f;
          z = __builtin_amdgcn_mfma_f32_32x32x16_bf16(__builtin_bit_cast(bf16x8, v0), qf0, z, 0, 0, 0);
          z = __builtin_amdgcn_mfma_f32_32x32x16_bf16(__builtin_bit_cast(bf16x8, v1), qf1, z, 0, 0, 0);
          S[u] = z;
        } else {
#pragma unroll
          for (int e = 0; e < 16; ++e) S[u][e] = -INFINITY;
        }
      }
      if (ch == 0 || ch == 2) {
#pragma unroll
        for (int e = 0; e < 16; ++e) {
          int rr = (e & 3) + 8 * (e >> 2) + 4 * h;
          if (ch == 0) { if (!(rr > c)) S[0][e] = -INFINITY; }
          else { if (!(rr <= c)) S[2][e] = -INFINITY; }
        }
      }
      float mx = mrun;
#pragma unroll
      for (int u = 0; u < 3; ++u)
#pragma unroll
        for (int e = 0; e < 16; e += 2) mx = fmaxf(fmaxf(mx, S[u][e]), S[u][e + 1]);
      mx = max_x32(mx);
      if (ch != 2) {
        const float alpha = __builtin_amdgcn_exp2f((mrun - mx) * C2);
#pragma unroll
        for (int e = 0; e < 16; ++e) O[e] *= alpha;
      }
      mrun = mx;
      const float nm = -mx * C2;
#pragma unroll
      for (int u = 0; u < 3; ++u) {
        const int kt = 3 * ch + u;
        if (kt >= kt_min) {
#pragma unroll
          for (int s = 0; s < 2; ++s) {
            u32x4 pk;
#pragma unroll
            for (int e = 0; e < 4; ++e) {
              float p0 = __builtin_amdgcn_exp2f(__builtin_fmaf(S[u][8 * s + 2 * e], C2, nm));
              float p1 = __builtin_amdgcn_exp2f(__builtin_fmaf(S[u][8 * s + 2 * e + 1], C2, nm));
              pk[e] = pack2(p0, p1);
            }
            u32x2 lo = lds_tru(vb + kt * 256 + s * 128);
            u32x2 hi = lds_tru(vb + kt * 256 + s * 128 + 64);
            O = __builtin_amdgcn_mfma_f32_32x32x16_bf16(cat_u(lo, hi), __builtin_bit_cast(bf16x8, pk), O, 0, 0, 0);
          }
        }
      }
    }
  }
  float l = __shfl(O[8], c + 32);
  float inv = 1.0f / l;
  {
    const int m = q0 + c;
    char* ob = ws + WS_A2 + (size_t)(m >> 4) * KS2 * 1024 + (m & 15) * 16;
#pragma unroll
    for (int grp = 0; grp < 3; ++grp) {
      if (grp < 2 || h == 0) {
        int kidx = head * 20 + 8 * grp + 4 * h;
        u32x2 o2 = {pack2(O[4 * grp] * inv, O[4 * grp + 1] * inv), pack2(O[4 * grp + 2] * inv, O[4 * grp + 3] * inv)};
        st8_dev(ob + (kidx >> 5) * 1024 + ((kidx >> 3) & 3) * 256 + ((kidx >> 2) & 1) * 8, o2);
      }
    }
  }
  WAITV(0);
  BARRIER();
  if (tid == 0)
    __hip_atomic_store((u64*)(ws + WS_FLAGO + pi * 128 + hp * 8), tag, __ATOMIC_RELAXED, __HIP_MEMORY_SCOPE_AGENT);
  STAMP(ws, 18);
}

DI void p3_compute(const u16* __restrict__ x, const u16* __restrict__ wo, const u16* __restrict__ wd,
                   char* __restrict__ ws, u16* __restrict__ out, int rt, int ct, lptr sm) {
  const int tid = threadIdx.x, lane = tid & 63;
  const int wid = __builtin_amdgcn_readfirstlane(tid >> 6);
  const int kh = wid >> 2, mh = (wid >> 1) & 1, nh = wid & 1;
  const int r = lane & 15, g = lane >> 4, q = (lane & 15) >> 2, pp = lane & 3;
  const unsigned xa = (unsigned)((mh * 3 * 4 + kh * 2) * 1024 + lane * 16);
  unsigned wa[2];
#pragma unroll
  for (int j = 0; j < 2; ++j) {
    int nt = 2 * nh + j;
    int f2 = (q >> 1) | ((g & 1) << 1);
    wa[j] = (unsigned)(24 * 1024 + (kh * 8 + g) * 1024 + q * 128 + ((nt ^ f2) * 32) + pp * 8);
  }
  f32x4 acc[3][2];
#pragma unroll
  for (int i = 0; i < 3; ++i)
#pragma unroll
    for (int j = 0; j < 2; ++j) acc[i][j] = f32x4{0.f, 0.f, 0.f, 0.f};
  u32x4 xres[2];
#pragma unroll
  for (int u = 0; u < 2; ++u) {
    int it = tid + 512 * u; it = it < 768 ? it : 0;
    int m = rt * 96 + (it >> 3); m = m > SEQ - 1 ? SEQ - 1 : m;
    xres[u] = *(const u32x4*)(x + (size_t)m * DM + ct * 64 + (it & 7) * 8);
  }
  unsigned boff[2];
#pragma unroll
  for (int i = 0; i < 2; ++i) {
    int kb = wid + 8 * i;
    int rr = lane >> 3, pc = lane & 7;
    int f = (((rr >> 1) & 1) << 1) | ((kb & 1) << 2);
    boff[i] = (unsigned)((kb * 8 + rr) * DM + ct * 64 + (pc ^ f) * 8) * 2u;
  }
  auto issue_b = [&](int st) {
    const char* bb = st < 12 ? (const char*)wd + (size_t)st * 128 * DM * 2 : (const char*)wo + (size_t)(st - 12) * 128 * DM * 2;
    lptr d = sm + ((st + 2) & 3) * P3_STAGE;
#pragma unroll
    for (int i = 0; i < 2; ++i) glds16(bb + boff[i], d + (24 + wid + 8 * i) * 1024);
  };
  issue_b(2);
#pragma unroll 1
  for (int t = 0; t < 17; ++t) {
    if (t < 15) WAITV(4); else if (t == 15) WAITV(2); else WAITV(0);
    BARRIER();
    if ((t & 3) == 0) STAMP(ws, 21 + t / 4);
    lptr sb = sm + ((t + 2) & 3) * P3_STAGE;
    u32x4 xf[2][3]; u32x2 wl[2][2], wh[2][2];
#pragma unroll
    for (int s = 0; s < 2; ++s) {
#pragma unroll
      for (int j = 0; j < 2; ++j) {
        wl[s][j] = lds_tru(sb + wa[j] + s * 4096);
        wh[s][j] = lds_tru(sb + wa[j] + s * 4096 + 512);
      }
#pragma unroll
      for (int i = 0; i < 3; ++i) xf[s][i] = lds_u128(sb + xa + i * 4096 + s * 1024);
    }
    __builtin_amdgcn_sched_barrier(0);
    if (t + 3 < 17) issue_b(t + 3);
    __builtin_amdgcn_sched_barrier(0);
#pragma unroll
    for (int s = 0; s < 2; ++s)
#pragma unroll
      for (int i = 0; i < 3; ++i)
#pragma unroll
        for (int j = 0; j < 2; ++j)
          acc[i][j] = __builtin_amdgcn_mfma_f32_16x16x32_bf16(cat_u(wl[s][j], wh[s][j]), __builtin_bit_cast(bf16x8, xf[s][i]), acc[i][j], 0, 0, 0);
  }
  BARRIER();
  STAMP(ws, 26);
  constexpr int RS = 68;
  {
    LAS float* red = (LAS float*)sm + kh * 96 * RS;
#pragma unroll
    for (int i = 0; i < 3; ++i)
#pragma unroll
      for (int j = 0; j < 2; ++j)
        *(LAS f32x4*)(red + ((mh * 3 + i) * 16 + r) * RS + (2 * nh + j) * 16 + 4 * g) = acc[i][j];
  }
  BARRIER();
#pragma unroll
  for (int u = 0; u < 2; ++u) {
    const int it = tid + 512 * u;
    int ml = it >> 3, c8 = it & 7;
    int m = rt * 96 + ml;
    if (it < 768 && m < SEQ) {
      LAS float* r0 = (LAS float*)sm + ml * RS + c8 * 8;
      LAS float* r1 = r0 + 96 * RS;
      f32x4 a0 = *(LAS f32x4*)r0, a1 = *(LAS f32x4*)(r0 + 4);
      f32x4 b0 = *(LAS f32x4*)r1, b1 = *(LAS f32x4*)(r1 + 4);
      const u32x4 xv = xres[u];
      float v[8];
#pragma unroll
      for (int e = 0; e < 4; ++e) { v[e] = a0[e] + b0[e]; v[4 + e] = a1[e] + b1[e]; }
      u32x4 o;
#pragma unroll
      for (int e = 0; e < 4; ++e) {
        float lo = __uint_as_float(xv[e] << 16), hi = __uint_as_float(xv[e] & 0xffff0000u);
        o[e] = pack2(lo + v[2 * e], hi + v[2 * e + 1]);
      }
      *(u32x4*)(out + (size_t)m * DM + ct * 64 + c8 * 8) = o;
    }
  }
  STAMP(ws, 27);
}

__global__ __launch_bounds__(768, 3) void k_fused(const u16* __restrict__ x, const u16* __restrict__ wqkv,
                                                  const u16* __restrict__ wo, const u16* __restrict__ wg,
                                                  const u16* __restrict__ wu, const u16* __restrict__ wd,
                                                  char* __restrict__ ws, u16* __restrict__ out) {
  extern __shared__ __attribute__((aligned(16))) char smem[];
  lptr sm = (lptr)smem;
  u64 tag = hw_dispatch_id() * 0x9E3779B97F4A7C15ull + (u64)(size_t)__builtin_amdgcn_dispatch_ptr() * 0xC2B2AE3D27D4EB4Full;
  tag ^= tag >> 29;
  tag = (tag | 1ull) & ~0x100ull;
  STAMP(ws, 0);
  const int wid = __builtin_amdgcn_readfirstlane(threadIdx.x >> 6);
  if (wid >= 8) {
    loader_main(x, wqkv, wo, wg, wu, wd, ws, sm, tag, wid - 8);
    return;
  }
  const Work W = get_work();
  if (W.p1type == 0) p1_compute<true>(x, wqkv, wg, wu, ws, W.p1rt, W.p1cg, sm, tag);
  else if (W.p1type == 1) p1_compute<false>(x, wqkv, wg, wu, ws, W.p1rt, W.p1cg, sm, tag);
  else {
    StatLoads SL;
    p0_issue(x, wqkv, wg, wu, SL);
    p0_compute(SL, ws, tag, sm + P1_STATS);
    BARRIER();
    p0_publish(ws, tag, sm + P1_STATS);
    if (threadIdx.x == 0) *(volatile LAS unsigned*)(sm + P2_GO) = 0u;
    BARRIER();
  }
  p2_compute(ws, W.pi, W.hp, sm, tag);
  if (W.p3rt >= 0) p3_compute(x, wo, wd, ws, out, W.p3rt, W.p3ct, sm);
}

extern "C" void kernel_launch(void* const* d_in, const int* in_sizes, int n_in,
                              void* d_out, int out_size, void* d_ws, size_t ws_size,
                              hipStream_t stream) {
  const u16* x = (const u16*)d_in[0];
  const u16* wqkv = (const u16*)d_in[1];
  const u16* wo = (const u16*)d_in[2];
  const u16* wg = (const u16*)d_in[4];
  const u16* wu = (const u16*)d_in[5];
  const u16* wd = (const u16*)d_in[6];
  (void)hipFuncSetAttribute((const void*)k_fused, hipFuncAttributeMaxDynamicSharedMemorySize, LDS_TOTAL);
  k_fused<<<256, 768, LDS_TOTAL, stream>>>(x, wqkv, wo, wg, wu, wd, (char*)d_ws, (u16*)d_out);
}
```

```cpp
#include <hip/hip_runtime.h>
#include <hip/hip_bf16.h>

typedef __attribute__((ext_vector_type(8))) __bf16 bf16x8;
typedef __attribute__((ext_vector_type(2))) __bf16 bf16x2;
typedef __attribute__((ext_vector_type(4))) short s16x4;
typedef __attribute__((ext_vector_type(4))) float f32x4;
typedef __attribute__((ext_vector_type(2))) float f32x2;
typedef __attribute__((ext_vector_type(16))) float f32x16;
typedef __attribute__((ext_vector_type(4))) unsigned u32x4;
typedef __attribute__((ext_vector_type(2))) unsigned u32x2;
#define LAS __attribute__((address_space(3)))
typedef LAS char* lptr;
typedef unsigned short u16;
typedef unsigned long long u64;

#define DI __device__ __forceinline__
extern "C" __device__ u64 hw_dispatch_id() __asm("llvm.amdgcn.dispatch.id");

constexpr int SEQ = 2048, DM = 640, FF = 1536;
constexpr int KS2 = 68;
constexpr size_t QKV_PLANE = (size_t)SEQ * 8;
constexpr size_t WS_QKV = 0;
constexpr size_t WS_A2 = WS_QKV + 3 * 32 * 5 * QKV_PLANE;
constexpr size_t WS_ROWST = WS_A2 + (size_t)128 * KS2 * 1024;
constexpr size_t WS_COLST = WS_ROWST + 2048 * 16;
constexpr size_t WS_FLAGQ = WS_COLST + 3 * 2496 * 16;
constexpr size_t WS_FLAGG = WS_FLAGQ + 8 * 128;
constexpr size_t WS_FLAGO = WS_FLAGG + 13 * 128;

DI void glds16(const void* g, lptr l) {
  __builtin_amdgcn_global_load_lds((const __attribute__((address_space(1))) unsigned*)g, (LAS unsigned*)l, 16, 0, 0);
}
DI void glds16_dev(const void* g, lptr l) {
  __builtin_amdgcn_global_load_lds((const __attribute__((address_space(1))) unsigned*)g, (LAS unsigned*)l, 16, 0, 16);
}
DI float dot2(unsigned a, unsigned b, float c) {
  return __builtin_amdgcn_fdot2_f32_bf16(__builtin_bit_cast(bf16x2, a), __builtin_bit_cast(bf16x2, b), c, false);
}
DI unsigned pack2(float a, float b) {
  bf16x2 v; v[0] = (__bf16)a; v[1] = (__bf16)b;
  return __builtin_bit_cast(unsigned, v);
}
#define STAMP(ws, i) do {} while (0)
#define WAITV(n) asm volatile("s_waitcnt vmcnt(" #n ")" ::: "memory")
#define BARRIER() do { asm volatile("" ::: "memory"); __builtin_amdgcn_s_waitcnt(0xC07F); __builtin_amdgcn_s_barrier(); asm volatile("" ::: "memory"); } while (0)
DI u32x4 lds_u128(lptr p) { return *(LAS u32x4*)p; }
DI u32x2 lds_tru(lptr p) { return __builtin_bit_cast(u32x2, __builtin_amdgcn_ds_read_tr16_b64_v4i16((LAS s16x4*)p)); }
DI bf16x8 cat_u(u32x2 lo, u32x2 hi) { u32x4 v = {lo[0], lo[1], hi[0], hi[1]}; return __builtin_bit_cast(bf16x8, v); }
DI lptr lp(unsigned a) { return (lptr)(size_t)a; }

DI void st8_dev(void* p, u32x2 v) {
  __hip_atomic_store((u64*)p, ((u64)v[1] << 32) | v[0], __ATOMIC_RELAXED, __HIP_MEMORY_SCOPE_AGENT);
}
DI void st16_dev(void* p, u32x4 v) {
  asm volatile("global_store_dwordx4 %0, %1, off sc1\n\ts_nop 1" :: "v"(p), "v"(v) : "memory");
}
DI u32x4 ld16_dev(const void* p) {
  u32x4 v;
  asm volatile("global_load_dwordx4 %0, %1, off sc1\n\ts_waitcnt vmcnt(0)" : "=&v"(v) : "v"(p) : "memory");
  return v;
}
DI u64 ld8_dev(const void* p) { return __hip_atomic_load((const u64*)p, __ATOMIC_RELAXED, __HIP_MEMORY_SCOPE_AGENT); }
constexpr unsigned SPIN_MAX = 1u << 22;
DI void wave_wait_flags(const char* p, bool act, u64 tag) {
  for (unsigned spins = 0; spins < SPIN_MAX; ++spins) {
    bool ok = true;
    if (act) ok = (ld8_dev(p) == tag);
    if (__all(ok)) break;
    __builtin_amdgcn_s_sleep(1);
  }
  asm volatile("" ::: "memory");
}

template <int N> DI float add_ror(float v) {
  return v + __builtin_bit_cast(float, __builtin_amdgcn_update_dpp(0, __builtin_bit_cast(int, v), 0x120 + N, 0xf, 0xf, false));
}
DI float add_x16(float v) {
  auto r = __builtin_amdgcn_permlane16_swap(__float_as_uint(v), __float_as_uint(v), false, false);
  return __uint_as_float(r[0]) + __uint_as_float(r[1]);
}
DI float add_x32(float v) {
  auto r = __builtin_amdgcn_permlane32_swap(__float_as_uint(v), __float_as_uint(v), false, false);
  return __uint_as_float(r[0]) + __uint_as_float(r[1]);
}
DI float max_x32(float v) {
  auto r = __builtin_amdgcn_permlane32_swap(__float_as_uint(v), __float_as_uint(v), false, false);
  return fmaxf(__uint_as_float(r[0]), __uint_as_float(r[1]));
}

constexpr int P1_STAGE = 52 * 1024;
constexpr int P1_STATS = 3 * P1_STAGE;
constexpr int LDS_TOTAL = 160 * 1024;

struct StatLoads { u32x4 r0, r1, c0, c1, c2, c3; };

DI void p0_issue(const u16* x, const u16* wqkv, const u16* wg, const u16* wu, StatLoads& L) {
  const int lane = threadIdx.x & 63, wid = threadIdx.x >> 6, b = blockIdx.x;
  const char* rp = (const char*)(x + (size_t)(8 * b + wid) * DM);
  const char* a0 = rp + lane * 16;
  const char* a1 = rp + 1024 + (lane & 15) * 16;
  int T = b < 234 ? b : 233;
  int u = T / 3, part = T - u * 3;
  const char* wb; int rs;
  if (u < 30) { wb = (const char*)(wqkv + u * 64); rs = 3 * DM * 2; }
  else if (u < 54) { wb = (const char*)(wg + (u - 30) * 64); rs = FF * 2; }
  else { wb = (const char*)(wu + (u - 54) * 64); rs = FF * 2; }
  const char* ca[4];
#pragma unroll
  for (int i = 0; i < 4; ++i) {
    int kr = part * 214 + (wid + 8 * i) * 8 + (lane >> 3);
    kr = kr > DM - 1 ? DM - 1 : kr;
    ca[i] = wb + (size_t)kr * rs + (lane & 7) * 16;
  }
  L.r0 = *(const u32x4*)a0; L.r1 = *(const u32x4*)a1;
  L.c0 = *(const u32x4*)ca[0]; L.c1 = *(const u32x4*)ca[1]; L.c2 = *(const u32x4*)ca[2]; L.c3 = *(const u32x4*)ca[3];
}
DI void p0_compute(StatLoads& L, char* ws, u64 tag, lptr red) {
  const int lane = threadIdx.x & 63, wid = threadIdx.x >> 6, b = blockIdx.x;
  const unsigned ONES = 0x3f803f80u;
  float s = 0.f, q = 0.f;
#pragma unroll
  for (int e = 0; e < 4; ++e) {
    unsigned a = L.r0[e], c = lane < 16 ? L.r1[e] : 0u;
    s = dot2(a, ONES, s); q = dot2(a, a, q);
    s = dot2(c, ONES, s); q = dot2(c, c, q);
  }
  s = add_ror<1>(s); q = add_ror<1>(q); s = add_ror<2>(s); q = add_ror<2>(q);
  s = add_ror<4>(s); q = add_ror<4>(q); s = add_ror<8>(s); q = add_ror<8>(q);
  s = add_x16(s); q = add_x16(q); s = add_x32(s); q = add_x32(q);
  {
    const float inv = 1.0f / DM;
    float m = s * inv;
    float v = fmaxf(q * inv - m * m, 0.f);
    float r = rsqrtf(v + 1e-5f);
    if (lane == 0) {
      u32x4 g = {__float_as_uint(m), __float_as_uint(r), (unsigned)tag, (unsigned)(tag >> 32)};
      st16_dev(ws + WS_ROWST + (size_t)(8 * b + wid) * 16, g);
    }
  }
  int T = b < 234 ? b : 233;
  int part = T % 3;
  int nrows = part < 2 ? 214 : 212;
  float cs[8];
#pragma unroll
  for (int e = 0; e < 8; ++e) cs[e] = 0.f;
#pragma unroll
  for (int i = 0; i < 4; ++i) {
    u32x4 v = i == 0 ? L.c0 : i == 1 ? L.c1 : i == 2 ? L.c2 : L.c3;
    bool ok = (wid + 8 * i) * 8 + (lane >> 3) < nrows;
#pragma unroll
    for (int e = 0; e < 4; ++e) {
      unsigned w = ok ? v[e] : 0u;
      cs[2 * e] += __uint_as_float(w << 16);
      cs[2 * e + 1] += __uint_as_float(w & 0xffff0000u);
    }
  }
#pragma unroll
  for (int e = 0; e < 8; ++e) cs[e] = add_x32(add_x16(add_ror<8>(cs[e])));
  if (lane < 8) {
    *(LAS f32x4*)(red + wid * 256 + lane * 32) = f32x4{cs[0], cs[1], cs[2], cs[3]};
    *(LAS f32x4*)(red + wid * 256 + lane * 32 + 16) = f32x4{cs[4], cs[5], cs[6], cs[7]};
  }
}
DI void p0_publish(char* ws, u64 tag, lptr red) {
  const int tid = threadIdx.x, b = blockIdx.x;
  if (tid < 32 && b < 234) {
    float c0 = 0.f, c1 = 0.f;
#pragma unroll
    for (int w = 0; w < 8; ++w) { f32x2 v = *(LAS f32x2*)(red + w * 256 + tid * 8); c0 += v[0]; c1 += v[1]; }
    int u = b / 3, part = b - u * 3;
    u32x4 g = {__float_as_uint(c0), __float_as_uint(c1), (unsigned)tag, (unsigned)(tag >> 32)};
    st16_dev(ws + WS_COLST + ((size_t)part * 2496 + u * 32 + tid) * 16, g);
  }
}

struct Work {
  int p1type;
  int p1rt, p1cg;
  int pi, hp;
  int p3rt, p3ct;
};
DI Work get_work() {
  Work w;
  const int e = blockIdx.x & 7, idx = blockIdx.x >> 3;
  const int p = e >> 1, half = e & 1;
  if (idx < 12) { w.p1type = 0; w.p1rt = half * 4 + idx / 3; w.p1cg = 3 * p + idx % 3; }
  else {
    int k = idx - 12, rt, c3;
    if (half == 0) { rt = k / 3; c3 = k % 3; }
    else { if (k == 0) { rt = 6; c3 = 2; } else { rt = 7 + (k - 1) / 3; c3 = (k - 1) % 3; } }
    w.p1type = rt <= 12 ? 1 : 2; w.p1rt = rt; w.p1cg = 3 * p + c3;
  }
  w.pi = idx >> 1; w.hp = e * 2 + (idx & 1);
  const int qd = e >> 1;
  const int start = (qd * 110 + 3) / 4, end = ((qd + 1) * 110 + 3) / 4;
  const int li = start + idx;
  w.p3rt = li < end ? li / 5 : -1; w.p3ct = (e & 1) * 5 + li % 5;
  return w;
}

constexpr int P2_PSTRIDE = 3072 + 32;
constexpr int P2_QOFF = 21 * P2_PSTRIDE;
constexpr int P3_STAGE = 40 * 1024;

constexpr int P2_GO = P2_QOFF + 10 * 1024;
DI void p2_stage(const char* ws, int pi, int hp, lptr sm, int w12) {
  const int lane = threadIdx.x & 63;
  const int kp_min = pi >= 2 ? 0 : 2 - pi;
#pragma unroll 1
  for (int pc = w12; pc < 70; pc += 12) {
    if (pc < 60) {
      int pl = pc / 3, kp = pc - pl * 3;
      if (kp >= kp_min) {
        int hh = pl / 10, w10 = pl - hh * 10;
        int which2 = 1 + (w10 >= 5), quad = w10 - (w10 >= 5 ? 5 : 0);
        const char* src = ws + WS_QKV + (size_t)((which2 * 32 + hp * 2 + hh) * 5 + quad) * QKV_PLANE +
                          (size_t)((pi - 2 + kp) * 128) * 8 + lane * 16;
        glds16_dev(src, sm + pl * P2_PSTRIDE + kp * 1024);
      }
    } else {
      int pl = pc - 60;
      const char* src = ws + WS_QKV + (size_t)(hp * 10 + pl) * QKV_PLANE + (size_t)(pi * 128) * 8 + lane * 16;
      glds16_dev(src, sm + P2_QOFF + pl * 1024);
    }
  }
  WAITV(0);
}

template <bool ISQ>
DI void p1_load(const u16* __restrict__ x, const u16* __restrict__ wqkv, const u16* __restrict__ wg,
                const u16* __restrict__ wu, char* __restrict__ ws, int rt, int cg, lptr sm, u64 tag, int lw) {
  constexpr int BM = ISQ ? 256 : 160, BN = ISQ ? 160 : 256;
  constexpr int APC = BM / 8, NA = APC / 4;
  const int lane = threadIdx.x & 63;
  const int row0 = rt * BM;
  unsigned poff[13];
#pragma unroll
  for (int i = 0; i < 13; ++i) {
    int p = lw + 4 * i;
    if (i < NA) {
      int rl = 8 * p + (lane >> 3), pc = lane & 7;
      int c = pc ^ ((rl >> 1) & 7);
      int gr = row0 + rl; gr = gr > SEQ - 1 ? SEQ - 1 : gr;
      poff[i] = (unsigned)(gr * DM + c * 8) * 2u;
    } else {
      int pb = p - APC;
      if (ISQ) {
        int ci = pb * 64 + lane;
        int kr = ci / 20, pc = ci - kr * 20;
        int c = pc ^ (((kr >> 3) & 1) << 1);
        poff[i] = (unsigned)(kr * (3 * DM) + cg * 160 + c * 8) * 2u;
      } else {
        int u = pb >> 3, kb = pb & 7;
        int r = lane >> 3, pc = lane & 7;
        int f = (((r >> 1) & 1) << 1) | ((kb & 1) << 2);
        int c = pc ^ f;
        poff[i] = (unsigned)((kb * 8 + r) * FF + cg * 128 + (u & 1) * 64 + c * 8) * 2u;
      }
    }
  }
  const char* xb = (const char*)x;
  const char* w0 = (const char*)(ISQ ? wqkv : wg);
  const char* w1 = (const char*)(ISQ ? wqkv : wu);
  auto issue = [&](int so) {
#pragma unroll
    for (int i = 0; i < 13; ++i) {
      const char* b = i < NA ? xb : ((i - NA) < 4 ? w0 : w1);
      glds16(b + poff[i], sm + so + (lw + 4 * i) * 1024);
    }
    xb += 128;
    w0 += ISQ ? 64 * 3 * DM * 2 : 64 * FF * 2;
    w1 += ISQ ? 64 * 3 * DM * 2 : 64 * FF * 2;
  };
  issue(0);
  issue(P1_STAGE);
  issue(2 * P1_STAGE);
  WAITV(26);
  BARRIER();
  int so = 0;
#pragma unroll 1
  for (int t = 0; t < 8; ++t) {
    WAITV(13);
    BARRIER();
    if (t < 7) issue(so);
    so = (so == 2 * P1_STAGE) ? 0 : so + P1_STAGE;
  }
  {
    LAS f32x2* rowstat = (LAS f32x2*)(sm + P1_STATS);
    LAS float* colsum = (LAS float*)(sm + P1_STATS + 2048);
    const int lt = lw * 64 + lane;
    int gr = row0 + (lt < BM ? lt : 0); gr = gr > SEQ - 1 ? SEQ - 1 : gr;
    const int lc = lt < BN / 2 ? lt : 0;
    const int gc = ISQ ? cg * 80 + lc : (lc < 64 ? 960 + cg * 64 + lc : 1728 + cg * 64 + (lc - 64));
    const char* g0 = ws + WS_ROWST + (size_t)gr * 16;
    const char* g1 = ws + WS_COLST + (size_t)gc * 16;
    const char* g2 = g1 + 2496 * 16;
    const char* g3 = g2 + 2496 * 16;
    u32x4 v0, v1, v2, v3;
    for (unsigned spins = 0; spins < SPIN_MAX; ++spins) {
      asm volatile("global_load_dwordx4 %0, %4, off sc1\n\tglobal_load_dwordx4 %1, %5, off sc1\n\t"
                   "global_load_dwordx4 %2, %6, off sc1\n\tglobal_load_dwordx4 %3, %7, off sc1\n\ts_waitcnt vmcnt(0)"
                   : "=&v"(v0), "=&v"(v1), "=&v"(v2), "=&v"(v3) : "v"(g0), "v"(g1), "v"(g2), "v"(g3) : "memory");
      const unsigned tl = (unsigned)tag, th = (unsigned)(tag >> 32);
      bool ok = v0[2] == tl && v0[3] == th && v1[2] == tl && v1[3] == th && v2[2] == tl && v2[3] == th && v3[2] == tl && v3[3] == th;
      if (__all(ok)) break;
      __builtin_amdgcn_s_sleep(1);
    }
    if (lt < BM) rowstat[lt] = f32x2{__uint_as_float(v0[0]), __uint_as_float(v0[1])};
    if (lt < BN / 2)
      *(LAS f32x2*)(colsum + 2 * lt) = f32x2{__uint_as_float(v1[0]) + __uint_as_float(v2[0]) + __uint_as_float(v3[0]),
                                             __uint_as_float(v1[1]) + __uint_as_float(v2[1]) + __uint_as_float(v3[1])};
  }
  BARRIER();
  BARRIER();
}

struct P3Load {
  unsigned aoff[6], boff[4];
};
DI void p3_load_setup(P3Load& L, int rt, int ct, int lw) {
  const int lane = threadIdx.x & 63;
#pragma unroll
  for (int i = 0; i < 6; ++i) {
    int p = lw + 4 * i;
    int gmt = rt * 6 + (p >> 2); gmt = gmt > 127 ? 127 : gmt;
    L.aoff[i] = (unsigned)((gmt * KS2 + (p & 3)) * 1024 + lane * 16);
  }
#pragma unroll
  for (int i = 0; i < 4; ++i) {
    int kb = lw + 4 * i;
    int r = lane >> 3, pc = lane & 7;
    int f = (((r >> 1) & 1) << 1) | ((kb & 1) << 2);
    int c = pc ^ f;
    L.boff[i] = (unsigned)((kb * 8 + r) * DM + ct * 64 + c * 8) * 2u;
  }
}
DI void p3_issue_a(const P3Load& L, const char* ws, int st, lptr sm, int lw) {
  const char* ab = ws + WS_A2 + (st < 12 ? (20 + 4 * st) : 4 * (st - 12)) * 1024;
  lptr d = sm + ((st + 2) & 3) * P3_STAGE;
#pragma unroll
  for (int i = 0; i < 6; ++i) glds16_dev(ab + L.aoff[i], d + (lw + 4 * i) * 1024);
}
DI void p3_issue_b(const P3Load& L, const u16* wo, const u16* wd, int st, lptr sm, int lw) {
  const char* bb = st < 12 ? (const char*)wd + (size_t)st * 128 * DM * 2 : (const char*)wo + (size_t)(st - 12) * 128 * DM * 2;
  lptr d = sm + ((st + 2) & 3) * P3_STAGE;
#pragma unroll
  for (int i = 0; i < 4; ++i) glds16(bb + L.boff[i], d + (24 + lw + 4 * i) * 1024);
}

DI void loader_main(const u16* x, const u16* wqkv, const u16* wo, const u16* wg, const u16* wu, const u16* wd,
                    char* ws, lptr sm, u64 tag, int lw) {
  const Work W = get_work();
  const int lane = threadIdx.x & 63;
  if (W.p1type == 0) p1_load<true>(x, wqkv, wg, wu, ws, W.p1rt, W.p1cg, sm, tag, lw);
  else if (W.p1type == 1) p1_load<false>(x, wqkv, wg, wu, ws, W.p1rt, W.p1cg, sm, tag, lw);
  else BARRIER();
  const bool has3 = W.p3rt >= 0;
  P3Load L3;
  p3_load_setup(L3, has3 ? W.p3rt : 0, W.p3ct, lw);
  if (has3) p3_issue_b(L3, wo, wd, 0, sm, lw);
  BARRIER();
  if (has3) p3_issue_b(L3, wo, wd, 1, sm, lw);
  const int pi = W.pi, hp = W.hp;
  {
    int which = lane >> 1, rsel = lane & 1;
    int rtile = rsel == 0 ? (pi >> 1) : (pi >= 2 ? (pi - 2) >> 1 : 0);
    wave_wait_flags(ws + WS_FLAGQ + rtile * 128 + (which * 4 + (hp >> 2)) * 8, lane < 6, tag);
    if (lw == 0 && lane == 0) *(volatile LAS unsigned*)(sm + P2_GO) = 1u;
    p2_stage(ws, pi, hp, sm, 8 + lw);
  }
  BARRIER();
  const int r_lo = W.p3rt * 96, r_hi = (r_lo + 95 > SEQ - 1) ? SEQ - 1 : r_lo + 95;
  if (has3) {
    int sel = lane >> 4, cgx = lane & 15;
    int gt = sel == 0 ? r_lo / 160 : r_hi / 160;
    wave_wait_flags(ws + WS_FLAGG + gt * 128 + cgx * 8, lane < 32 && cgx < 12, tag);
    p3_issue_a(L3, ws, 0, sm, lw);
    p3_issue_a(L3, ws, 1, sm, lw);
  }
  BARRIER();
  if (!has3) return;
  p3_issue_a(L3, ws, 2, sm, lw);
#pragma unroll 1
  for (int t = 0; t < 17; ++t) {
    if (t < 15) WAITV(12); else if (t == 15) WAITV(6); else WAITV(0);
    BARRIER();
    if (t + 3 < 17) {
      if (t + 3 == 12) {
        int sel = lane >> 4, hpx = lane & 15;
        int pn = sel == 0 ? r_lo >> 7 : r_hi >> 7;
        wave_wait_flags(ws + WS_FLAGO + pn * 128 + hpx * 8, lane < 32, tag);
      }
      p3_issue_a(L3, ws, t + 3, sm, lw);
    }
  }
}

template <bool ISQ>
DI void p1_compute(const u16* __restrict__ x, const u16* __restrict__ wqkv, const u16* __restrict__ wg,
                   const u16* __restrict__ wu, char* __restrict__ ws, int rt, int cg, lptr sm, u64 tag) {
  constexpr int BM = ISQ ? 256 : 160;
  constexpr int MT = ISQ ? 4 : 5, NT = ISQ ? 5 : 4;
  constexpr int NWN = ISQ ? 2 : 4;
  constexpr int ABYTES = BM * 128;
  const int tid = threadIdx.x, lane = tid & 63;
  const int wid = __builtin_amdgcn_readfirstlane(tid >> 6);
  const int wm = wid / NWN, wn = wid % NWN;
  const int row0 = rt * BM;
  {
    StatLoads SL;
    p0_issue(x, wqkv, wg, wu, SL);
    p0_compute(SL, ws, tag, sm + P1_STATS);
  }
  const int r = lane & 15, g = lane >> 4, q = (lane & 15) >> 2, pp = lane & 3;
  unsigned xa0 = (unsigned)(size_t)sm + (unsigned)((wm * MT * 16 + r) * 128 + ((g ^ (r >> 1)) * 16));
  unsigned xa1 = xa0 ^ 64u;
  unsigned wa[2];
  if (ISQ) {
#pragma unroll
    for (int j = 0; j < 2; ++j) {
      int nt = 5 * wn + j;
      wa[j] = (unsigned)(size_t)sm + (unsigned)(ABYTES + (8 * g + q) * 320 + ((nt ^ (g & 1)) * 32) + pp * 8);
    }
  } else {
#pragma unroll
    for (int j = 0; j < 2; ++j) {
      int u = wn >> 1;
      int nt = 2 * (wn & 1) + j;
      int f2 = (q >> 1) | ((g & 1) << 1);
      wa[j] = (unsigned)(size_t)sm + (unsigned)(ABYTES + (u * 8 + g) * 1024 + q * 128 + ((nt ^ f2) * 32) + pp * 8);
    }
  }
  constexpr int W_T = ISQ ? 4 * 320 : 512;
  constexpr int W_S = ISQ ? 32 * 320 : 4096;
  constexpr int W_J = ISQ ? 64 : 16384;

  f32x4 acc[MT][NT];
#pragma unroll
  for (int i = 0; i < MT; ++i)
#pragma unroll
    for (int j = 0; j < NT; ++j) acc[i][j] = f32x4{0.f, 0.f, 0.f, 0.f};

  u32x4 xA[MT], xB[MT];
  u32x2 wAl[NT], wAh[NT], wBl[NT], wBh[NT];
  constexpr int NRD = 2 * NT + MT;
  auto rd = [&](int ri, u32x4* xn, u32x2* wl, u32x2* wh, unsigned xad, int s) {
    if (ri == 2) { xn[0] = lds_u128(lp(xad)); return; }
    int k = ri < 2 ? ri : ri - 1;
    if (k < 2 * NT) {
      int j = k >> 1;
      u32x2 v = lds_tru(lp(wa[j & 1]) + (j >> 1) * W_J + s * W_S + (k & 1) * W_T);
      if (k & 1) wh[j] = v; else wl[j] = v;
    } else {
      int i = k - 2 * NT + 1;
      xn[i] = lds_u128(lp(xad) + i * 2048);
    }
  };
  auto kstep = [&](const u32x4* xc, const u32x2* wcl, const u32x2* wch, u32x4* xn, u32x2* wnl, u32x2* wnh,
                   unsigned xad, int sn) {
#pragma unroll
    for (int k = 0; k < 20; ++k) {
      const int i = k / NT, j = k % NT;
      acc[i][j] = __builtin_amdgcn_mfma_f32_16x16x32_bf16(cat_u(wcl[j], wch[j]), __builtin_bit_cast(bf16x8, xc[i]), acc[i][j], 0, 0, 0);
      __builtin_amdgcn_sched_barrier(0);
      if (k < NRD) rd(k, xn, wnl, wnh, xad, sn);
      __builtin_amdgcn_sched_barrier(0);
    }
  };

  BARRIER();
  STAMP(ws, 1);
  p0_publish(ws, tag, sm + P1_STATS);
#pragma unroll
  for (int ri = 0; ri < NRD; ++ri) rd(ri, xA, wAl, wAh, xa0, 0);
  int so_cur = 0;
#pragma unroll 1
  for (int t = 0; t < 10; ++t) {
    const int so_n = (so_cur == 2 * P1_STAGE) ? 0 : so_cur + P1_STAGE;
    const int dlt = so_n - so_cur;
    kstep(xA, wAl, wAh, xB, wBl, wBh, xa1, 1);
    xa0 += dlt; xa1 += dlt; wa[0] += dlt; wa[1] += dlt;
    BARRIER();
    STAMP(ws, 2 + t);
    kstep(xB, wBl, wBh, xA, wAl, wAh, xa0, 0);
    so_cur = so_n;
  }
  STAMP(ws, 12);

  LAS f32x2* rowstat = (LAS f32x2*)(sm + P1_STATS);
  LAS float* colsum = (LAS float*)(sm + P1_STATS + 2048);
  f32x4 csv[NT];
#pragma unroll
  for (int j = 0; j < NT; ++j) {
    int nb = ISQ ? 5 * wn + j : (j >> 1) * 8 + 2 * wn + (j & 1);
    csv[j] = *(LAS f32x4*)(colsum + nb * 16 + 4 * g);
  }
#pragma unroll
  for (int i = 0; i < MT; ++i) {
    const int ml = wm * MT * 16 + i * 16 + r;
    const f32x2 st = rowstat[ml];
    const int m = row0 + ml;
    if (ISQ) {
      const int which = cg >> 2;
#pragma unroll
      for (int j = 0; j < NT; ++j) {
        int col = (5 * wn + j) * 16 + 4 * g;
        int hl = col / 20, quad = (col - hl * 20) >> 2;
        int head = (cg & 3) * 8 + hl;
        f32x4 y;
#pragma unroll
        for (int e = 0; e < 4; ++e) y[e] = st[1] * (acc[i][j][e] - st[0] * csv[j][e]);
        u32x2 o2 = {pack2(y[0], y[1]), pack2(y[2], y[3])};
        st8_dev(ws + WS_QKV + (size_t)((which * 32 + head) * 5 + quad) * QKV_PLANE + (size_t)m * 8, o2);
      }
    } else {
      if (m < SEQ) {
#pragma unroll
        for (int tp = 0; tp < 2; ++tp) {
          float hv[4];
#pragma unroll
          for (int e = 0; e < 4; ++e) {
            float gt = st[1] * (acc[i][tp][e] - st[0] * csv[tp][e]);
            float up = st[1] * (acc[i][tp + 2][e] - st[0] * csv[tp + 2][e]);
            float sg = __builtin_amdgcn_rcpf(1.0f + __expf(-gt));
            hv[e] = gt * sg * up;
          }
          u32x2 o2 = {pack2(hv[0], hv[1]), pack2(hv[2], hv[3])};
          int ks = 20 + 4 * cg + wn;
          int gg = 2 * tp + (g >> 1);
          st8_dev(ws + WS_A2 + ((size_t)(m >> 4) * KS2 + ks) * 1024 + (gg * 16 + r) * 16 + (g & 1) * 8, o2);
        }
      }
    }
  }
  if (tid == 0) *(volatile LAS unsigned*)(sm + P2_GO) = 0u;
  WAITV(0);
  BARRIER();
  if (tid == 0)
    __hip_atomic_store((u64*)(ws + (ISQ ? WS_FLAGQ : WS_FLAGG) + rt * 128 + cg * 8), tag, __ATOMIC_RELAXED, __HIP_MEMORY_SCOPE_AGENT);
  STAMP(ws, 14);
}

DI void p2_compute(char* __restrict__ ws, int pi, int hp, lptr sm, u64 tag) {
  const int tid = threadIdx.x, lane = tid & 63;
  const int wid = __builtin_amdgcn_readfirstlane(tid >> 6);
  if (tid < 384) *(LAS u32x2*)(sm + 20 * P2_PSTRIDE + tid * 8) = u32x2{0x00003f80u, 0u};
  for (unsigned spins = 0; spins < (SPIN_MAX << 6); ++spins) {
    if (*(volatile LAS unsigned*)(sm + P2_GO) != 0u) break;
    __builtin_amdgcn_s_sleep(2);
  }
  asm volatile("" ::: "memory");
  p2_stage(ws, pi, hp, sm, wid);
  BARRIER();
  STAMP(ws, 17);
  const int qt = wid & 3, hh = wid >> 2;
  const int head = hp * 2 + hh;
  const int q0 = pi * 128 + qt * 32;
  const int c = lane & 31, h = lane >> 5;
  bf16x8 qf0, qf1;
  {
    lptr qb = sm + P2_QOFF + hh * 5 * 1024 + (qt * 32 + c) * 8;
    u32x2 a = *(LAS u32x2*)(qb + (2 * h) * 1024);
    u32x2 b = *(LAS u32x2*)(qb + (2 * h + 1) * 1024);
    u32x2 d = *(LAS u32x2*)(qb + 4 * 1024);
    u32x4 v0 = {a[0], a[1], b[0], b[1]};
    u32x4 v1 = {h ? 0u : d[0], h ? 0u : d[1], 0u, 0u};
    qf0 = __builtin_bit_cast(bf16x8, v0);
    qf1 = __builtin_bit_cast(bf16x8, v1);
  }
  const int kt_min = q0 >= 256 ? 0 : (256 - q0) >> 5;
  lptr kb = sm + (hh * 10) * P2_PSTRIDE + (qt * 32 + c) * 8;
  lptr vb;
  {
    const int blk = (lane >> 4) & 1, q = (lane & 15) >> 2, pp = lane & 3;
    int pl = blk ? (pp == 0 ? hh * 10 + 9 : 20) : hh * 10 + 5 + pp;
    vb = sm + pl * P2_PSTRIDE + (qt * 32 + 4 * h + q) * 8;
  }
  const float C2 = 0.22360679774997896f * 1.4426950408889634f;
  f32x16 O;
#pragma unroll
  for (int e = 0; e < 16; ++e) O[e] = 0.f;
  float mrun = -INFINITY;
#pragma unroll
  for (int ch = 2; ch >= 0; --ch) {
    if (3 * ch + 2 >= kt_min) {
      f32x16 S[3];
#pragma unroll
      for (int u = 0; u < 3; ++u) {
        const int kt = 3 * ch + u;
        if (kt >= kt_min) {
          u32x2 a = *(LAS u32x2*)(kb + (2 * h) * P2_PSTRIDE + kt * 256);
          u32x2 b = *(LAS u32x2*)(kb + (2 * h + 1) * P2_PSTRIDE + kt * 256);
          u32x2 d = *(LAS u32x2*)(kb + 4 * P2_PSTRIDE + kt * 256);
          u32x4 v0 = {a[0], a[1], b[0], b[1]};
          u32x4 v1 = {d[0], d[1], 0u, 0u};
          f32x16 z;
#pragma unroll
          for (int e = 0; e < 16; ++e) z[e] = 0.f;
          z = __builtin_amdgcn_mfma_f32_32x32x16_bf16(__builtin_bit_cast(bf16x8, v0), qf0, z, 0, 0, 0);
          z = __builtin_amdgcn_mfma_f32_32x32x16_bf16(__builtin_bit_cast(bf16x8, v1), qf1, z, 0, 0, 0);
          S[u] = z;
        } else {
#pragma unroll
          for (int e = 0; e < 16; ++e) S[u][e] = -INFINITY;
        }
      }
      if (ch == 0 || ch == 2) {
#pragma unroll
        for (int e = 0; e < 16; ++e) {
          int rr = (e & 3) + 8 * (e >> 2) + 4 * h;
          if (ch == 0) { if (!(rr > c)) S[0][e] = -INFINITY; }
          else { if (!(rr <= c)) S[2][e] = -INFINITY; }
        }
      }
      float mx = mrun;
#pragma unroll
      for (int u = 0; u < 3; ++u)
#pragma unroll
        for (int e = 0; e < 16; e += 2) mx = fmaxf(fmaxf(mx, S[u][e]), S[u][e + 1]);
      mx = max_x32(mx);
      if (ch != 2) {
        const float alpha = __builtin_amdgcn_exp2f((mrun - mx) * C2);
#pragma unroll
        for (int e = 0; e < 16; ++e) O[e] *= alpha;
      }
      mrun = mx;
      const float nm = -mx * C2;
#pragma unroll
      for (int u = 0; u < 3; ++u) {
        const int kt = 3 * ch + u;
        if (kt >= kt_min) {
#pragma unroll
          for (int s = 0; s < 2; ++s) {
            u32x4 pk;
#pragma unroll
            for (int e = 0; e < 4; ++e) {
              float p0 = __builtin_amdgcn_exp2f(__builtin_fmaf(S[u][8 * s + 2 * e], C2, nm));
              float p1 = __builtin_amdgcn_exp2f(__builtin_fmaf(S[u][8 * s + 2 * e + 1], C2, nm));
              pk[e] = pack2(p0, p1);
            }
            u32x2 lo = lds_tru(vb + kt * 256 + s * 128);
            u32x2 hi = lds_tru(vb + kt * 256 + s * 128 + 64);
            O = __builtin_amdgcn_mfma_f32_32x32x16_bf16(cat_u(lo, hi), __builtin_bit_cast(bf16x8, pk), O, 0, 0, 0);
          }
        }
      }
    }
  }
  float l = __shfl(O[8], c + 32);
  float inv = 1.0f / l;
  {
    const int m = q0 + c;
    char* ob = ws + WS_A2 + (size_t)(m >> 4) * KS2 * 1024 + (m & 15) * 16;
#pragma unroll
    for (int grp = 0; grp < 3; ++grp) {
      if (grp < 2 || h == 0) {
        int kidx = head * 20 + 8 * grp + 4 * h;
        u32x2 o2 = {pack2(O[4 * grp] * inv, O[4 * grp + 1] * inv), pack2(O[4 * grp + 2] * inv, O[4 * grp + 3] * inv)};
        st8_dev(ob + (kidx >> 5) * 1024 + ((kidx >> 3) & 3) * 256 + ((kidx >> 2) & 1) * 8, o2);
      }
    }
  }
  WAITV(0);
  BARRIER();
  if (tid == 0)
    __hip_atomic_store((u64*)(ws + WS_FLAGO + pi * 128 + hp * 8), tag, __ATOMIC_RELAXED, __HIP_MEMORY_SCOPE_AGENT);
  STAMP(ws, 18);
}

DI void p3_compute(const u16* __restrict__ x, const u16* __restrict__ wo, const u16* __restrict__ wd,
                   char* __restrict__ ws, u16* __restrict__ out, int rt, int ct, lptr sm) {
  const int tid = threadIdx.x, lane = tid & 63;
  const int wid = __builtin_amdgcn_readfirstlane(tid >> 6);
  const int kh = wid >> 2, mh = (wid >> 1) & 1, nh = wid & 1;
  const int r = lane & 15, g = lane >> 4, q = (lane & 15) >> 2, pp = lane & 3;
  const unsigned xa = (unsigned)((mh * 3 * 4 + kh * 2) * 1024 + lane * 16);
  unsigned wa[2];
#pragma unroll
  for (int j = 0; j < 2; ++j) {
    int nt = 2 * nh + j;
    int f2 = (q >> 1) | ((g & 1) << 1);
    wa[j] = (unsigned)(24 * 1024 + (kh * 8 + g) * 1024 + q * 128 + ((nt ^ f2) * 32) + pp * 8);
  }
  f32x4 acc[3][2];
#pragma unroll
  for (int i = 0; i < 3; ++i)
#pragma unroll
    for (int j = 0; j < 2; ++j) acc[i][j] = f32x4{0.f, 0.f, 0.f, 0.f};
  u32x4 xres[2];
#pragma unroll
  for (int u = 0; u < 2; ++u) {
    int it = tid + 512 * u; it = it < 768 ? it : 0;
    int m = rt * 96 + (it >> 3); m = m > SEQ - 1 ? SEQ - 1 : m;
    xres[u] = *(const u32x4*)(x + (size_t)m * DM + ct * 64 + (it & 7) * 8);
  }
  unsigned boff[2];
#pragma unroll
  for (int i = 0; i < 2; ++i) {
    int kb = wid + 8 * i;
    int rr = lane >> 3, pc = lane & 7;
    int f = (((rr >> 1) & 1) << 1) | ((kb & 1) << 2);
    boff[i] = (unsigned)((kb * 8 + rr) * DM + ct * 64 + (pc ^ f) * 8) * 2u;
  }
  auto issue_b = [&](int st) {
    const char* bb = st < 12 ? (const char*)wd + (size_t)st * 128 * DM * 2 : (const char*)wo + (size_t)(st - 12) * 128 * DM * 2;
    lptr d = sm + ((st + 2) & 3) * P3_STAGE;
#pragma unroll
    for (int i = 0; i < 2; ++i) glds16(bb + boff[i], d + (24 + wid + 8 * i) * 1024);
  };
  issue_b(2);
#pragma unroll 1
  for (int t = 0; t < 17; ++t) {
    if (t < 15) WAITV(4); else if (t == 15) WAITV(2); else WAITV(0);
    BARRIER();
    if ((t & 3) == 0) STAMP(ws, 21 + t / 4);
    lptr sb = sm + ((t + 2) & 3) * P3_STAGE;
    u32x4 xf[2][3]; u32x2 wl[2][2], wh[2][2];
#pragma unroll
    for (int s = 0; s < 2; ++s) {
#pragma unroll
      for (int j = 0; j < 2; ++j) {
        wl[s][j] = lds_tru(sb + wa[j] + s * 4096);
        wh[s][j] = lds_tru(sb + wa[j] + s * 4096 + 512);
      }
#pragma unroll
      for (int i = 0; i < 3; ++i) xf[s][i] = lds_u128(sb + xa + i * 4096 + s * 1024);
    }
    __builtin_amdgcn_sched_barrier(0);
    if (t + 3 < 17) issue_b(t + 3);
    __builtin_amdgcn_sched_barrier(0);
#pragma unroll
    for (int s = 0; s < 2; ++s)
#pragma unroll
      for (int i = 0; i < 3; ++i)
#pragma unroll
        for (int j = 0; j < 2; ++j)
          acc[i][j] = __builtin_amdgcn_mfma_f32_16x16x32_bf16(cat_u(wl[s][j], wh[s][j]), __builtin_bit_cast(bf16x8, xf[s][i]), acc[i][j], 0, 0, 0);
  }
  BARRIER();
  STAMP(ws, 26);
  constexpr int RS = 68;
  {
    LAS float* red = (LAS float*)sm + kh * 96 * RS;
#pragma unroll
    for (int i = 0; i < 3; ++i)
#pragma unroll
      for (int j = 0; j < 2; ++j)
        *(LAS f32x4*)(red + ((mh * 3 + i) * 16 + r) * RS + (2 * nh + j) * 16 + 4 * g) = acc[i][j];
  }
  BARRIER();
#pragma unroll
  for (int u = 0; u < 2; ++u) {
    const int it = tid + 512 * u;
    int ml = it >> 3, c8 = it & 7;
    int m = rt * 96 + ml;
    if (it < 768 && m < SEQ) {
      LAS float* r0 = (LAS float*)sm + ml * RS + c8 * 8;
      LAS float* r1 = r0 + 96 * RS;
      f32x4 a0 = *(LAS f32x4*)r0, a1 = *(LAS f32x4*)(r0 + 4);
      f32x4 b0 = *(LAS f32x4*)r1, b1 = *(LAS f32x4*)(r1 + 4);
      const u32x4 xv = xres[u];
      float v[8];
#pragma unroll
      for (int e = 0; e < 4; ++e) { v[e] = a0[e] + b0[e]; v[4 + e] = a1[e] + b1[e]; }
      u32x4 o;
#pragma unroll
      for (int e = 0; e < 4; ++e) {
        float lo = __uint_as_float(xv[e] << 16), hi = __uint_as_float(xv[e] & 0xffff0000u);
        o[e] = pack2(lo + v[2 * e], hi + v[2 * e + 1]);
      }
      *(u32x4*)(out + (size_t)m * DM + ct * 64 + c8 * 8) = o;
    }
  }
  STAMP(ws, 27);
}

__global__ __launch_bounds__(768, 3) void k_fused(const u16* __restrict__ x, const u16* __restrict__ wqkv,
                                                  const u16* __restrict__ wo, const u16* __restrict__ wg,
                                                  const u16* __restrict__ wu, const u16* __restrict__ wd,
                                                  char* __restrict__ ws, u16* __restrict__ out) {
  extern __shared__ __attribute__((aligned(16))) char smem[];
  lptr sm = (lptr)smem;
  u64 tag = hw_dispatch_id() * 0x9E3779B97F4A7C15ull + (u64)(size_t)__builtin_amdgcn_dispatch_ptr() * 0xC2B2AE3D27D4EB4Full;
  tag ^= tag >> 29;
  tag = (tag | 1ull) & ~0x100ull;
  STAMP(ws, 0);
  const int wid = __builtin_amdgcn_readfirstlane(threadIdx.x >> 6);
  {
    u64 pc; asm volatile("s_getpc_b64 %0" : "=s"(pc));
    if (wid == 9) {
      const char* cp = (const char*)(pc & ~(u64)63) + (blockIdx.x >> 3) * 960 + (threadIdx.x & 63) * 16;
      if ((threadIdx.x & 63) < 60) { u32x4 v = *(const u32x4*)cp; asm volatile("" :: "v"(v)); }
    }
  }
  {
    if (wid == 10) {
      const int lane = threadIdx.x & 63;
      const size_t o = (size_t)blockIdx.x * 10880;
      unsigned acc = 0;
#pragma unroll
      for (int i = 0; i < 2; ++i) {
        size_t a = o + (size_t)(lane + 64 * i) * 128;
        if (lane + 64 * i < 85) { const char* p = a < 819200 ? (const char*)wo + a : (const char*)wd + (a - 819200); acc += *(const unsigned*)p; }
      }
      acc += *(const unsigned*)(ws + (size_t)(blockIdx.x) * 65536 + lane * 4);
      asm volatile("" :: "v"(acc));
    }
  }
  if (wid >= 8) {
    loader_main(x, wqkv, wo, wg, wu, wd, ws, sm, tag, wid - 8);
    return;
  }
  const Work W = get_work();
  if (W.p1type == 0) p1_compute<true>(x, wqkv, wg, wu, ws, W.p1rt, W.p1cg, sm, tag);
  else if (W.p1type == 1) p1_compute<false>(x, wqkv, wg, wu, ws, W.p1rt, W.p1cg, sm, tag);
  else {
    StatLoads SL;
    p0_issue(x, wqkv, wg, wu, SL);
    p0_compute(SL, ws, tag, sm + P1_STATS);
    BARRIER();
    p0_publish(ws, tag, sm + P1_STATS);
    if (threadIdx.x == 0) *(volatile LAS unsigned*)(sm + P2_GO) = 0u;
    BARRIER();
  }
  p2_compute(ws, W.pi, W.hp, sm, tag);
  if (W.p3rt >= 0) p3_compute(x, wo, wd, ws, out, W.p3rt, W.p3ct, sm);
}

extern "C" void kernel_launch(void* const* d_in, const int* in_sizes, int n_in,
                              void* d_out, int out_size, void* d_ws, size_t ws_size,
                              hipStream_t stream) {
  const u16* x = (const u16*)d_in[0];
  const u16* wqkv = (const u16*)d_in[1];
  const u16* wo = (const u16*)d_in[2];
  const u16* wg = (const u16*)d_in[4];
  const u16* wu = (const u16*)d_in[5];
  const u16* wd = (const u16*)d_in[6];
  (void)hipFuncSetAttribute((const void*)k_fused, hipFuncAttributeMaxDynamicSharedMemorySize, LDS_TOTAL);
  k_fused<<<256, 768, LDS_TOTAL, stream>>>(x, wqkv, wo, wg, wu, wd, (char*)d_ws, (u16*)d_out);
}
```

```cpp
#include <hip/hip_runtime.h>
#include <hip/hip_bf16.h>
#include <type_traits>

typedef __attribute__((ext_vector_type(8))) __bf16 bf16x8;
typedef __attribute__((ext_vector_type(2))) __bf16 bf16x2;
typedef __attribute__((ext_vector_type(4))) short s16x4;
typedef __attribute__((ext_vector_type(4))) float f32x4;
typedef __attribute__((ext_vector_type(2))) float f32x2;
typedef __attribute__((ext_vector_type(16))) float f32x16;
typedef __attribute__((ext_vector_type(4))) unsigned u32x4;
typedef __attribute__((ext_vector_type(2))) unsigned u32x2;
#define LAS __attribute__((address_space(3)))
typedef LAS char* lptr;
typedef unsigned short u16;
typedef unsigned long long u64;

#define DI __device__ __forceinline__
extern "C" __device__ u64 hw_dispatch_id() __asm("llvm.amdgcn.dispatch.id");

constexpr int SEQ = 2048, DM = 640, FF = 1536;
constexpr int KS2 = 68;
constexpr size_t QKV_PLANE = (size_t)SEQ * 8;
constexpr size_t WS_QKV = 0;
constexpr size_t WS_A2 = WS_QKV + 3 * 32 * 5 * QKV_PLANE;
constexpr size_t WS_ROWST = WS_A2 + (size_t)128 * KS2 * 1024;
constexpr size_t WS_COLST = WS_ROWST + 2048 * 16;
constexpr size_t WS_FLAGQ = WS_COLST + 3 * 2496 * 16;
constexpr size_t WS_FLAGG = WS_FLAGQ + 8 * 128;
constexpr size_t WS_FLAGO = WS_FLAGG + 13 * 128;

DI void glds16(const void* g, lptr l) {
  __builtin_amdgcn_global_load_lds((const __attribute__((address_space(1))) unsigned*)g, (LAS unsigned*)l, 16, 0, 0);
}
DI void glds16_dev(const void* g, lptr l) {
  __builtin_amdgcn_global_load_lds((const __attribute__((address_space(1))) unsigned*)g, (LAS unsigned*)l, 16, 0, 16);
}
DI float dot2(unsigned a, unsigned b, float c) {
  return __builtin_amdgcn_fdot2_f32_bf16(__builtin_bit_cast(bf16x2, a), __builtin_bit_cast(bf16x2, b), c, false);
}
DI unsigned pack2(float a, float b) {
  bf16x2 v; v[0] = (__bf16)a; v[1] = (__bf16)b;
  return __builtin_bit_cast(unsigned, v);
}
#define STAMP(ws, i) do {} while (0)
#define WAITV(n) asm volatile("s_waitcnt vmcnt(" #n ")" ::: "memory")
#define BARRIER() do { asm volatile("" ::: "memory"); __builtin_amdgcn_s_waitcnt(0xC07F); __builtin_amdgcn_s_barrier(); asm volatile("" ::: "memory"); } while (0)
DI u32x4 lds_u128(lptr p) { return *(LAS u32x4*)p; }
DI u32x2 lds_tru(lptr p) { return __builtin_bit_cast(u32x2, __builtin_amdgcn_ds_read_tr16_b64_v4i16((LAS s16x4*)p)); }
DI bf16x8 cat_u(u32x2 lo, u32x2 hi) { u32x4 v = {lo[0], lo[1], hi[0], hi[1]}; return __builtin_bit_cast(bf16x8, v); }
DI lptr lp(unsigned a) { return (lptr)(size_t)a; }

DI void st8_dev(void* p, u32x2 v) {
  __hip_atomic_store((u64*)p, ((u64)v[1] << 32) | v[0], __ATOMIC_RELAXED, __HIP_MEMORY_SCOPE_AGENT);
}
DI void st16_dev(void* p, u32x4 v) {
  asm volatile("global_store_dwordx4 %0, %1, off sc1\n\ts_nop 1" :: "v"(p), "v"(v) : "memory");
}
DI u32x4 ld16_dev(const void* p) {
  u32x4 v;
  asm volatile("global_load_dwordx4 %0, %1, off sc1\n\ts_waitcnt vmcnt(0)" : "=&v"(v) : "v"(p) : "memory");
  return v;
}
DI u64 ld8_dev(const void* p) { return __hip_atomic_load((const u64*)p, __ATOMIC_RELAXED, __HIP_MEMORY_SCOPE_AGENT); }
constexpr unsigned SPIN_MAX = 1u << 22;
DI void wave_wait_flags(const char* p, bool act, u64 tag) {
  for (unsigned spins = 0; spins < SPIN_MAX; ++spins) {
    bool ok = true;
    if (act) ok = (ld8_dev(p) == tag);
    if (__all(ok)) break;
    __builtin_amdgcn_s_sleep(1);
  }
  asm volatile("" ::: "memory");
}

template <int N> DI float add_ror(float v) {
  return v + __builtin_bit_cast(float, __builtin_amdgcn_update_dpp(0, __builtin_bit_cast(int, v), 0x120 + N, 0xf, 0xf, false));
}
DI float add_x16(float v) {
  auto r = __builtin_amdgcn_permlane16_swap(__float_as_uint(v), __float_as_uint(v), false, false);
  return __uint_as_float(r[0]) + __uint_as_float(r[1]);
}
DI float add_x32(float v) {
  auto r = __builtin_amdgcn_permlane32_swap(__float_as_uint(v), __float_as_uint(v), false, false);
  return __uint_as_float(r[0]) + __uint_as_float(r[1]);
}
DI float max_x32(float v) {
  auto r = __builtin_amdgcn_permlane32_swap(__float_as_uint(v), __float_as_uint(v), false, false);
  return fmaxf(__uint_as_float(r[0]), __uint_as_float(r[1]));
}

constexpr int P1_STAGE = 52 * 1024;
constexpr int P1_STATS = 3 * P1_STAGE;
constexpr int LDS_TOTAL = 160 * 1024;

struct StatLoads { u32x4 r0, r1, c0, c1, c2, c3; };

DI void p0_issue(const u16* x, const u16* wqkv, const u16* wg, const u16* wu, StatLoads& L) {
  const int lane = threadIdx.x & 63, wid = threadIdx.x >> 6, b = blockIdx.x;
  const char* rp = (const char*)(x + (size_t)(8 * b + wid) * DM);
  const char* a0 = rp + lane * 16;
  const char* a1 = rp + 1024 + (lane & 15) * 16;
  int T = b < 234 ? b : 233;
  int u = T / 3, part = T - u * 3;
  const char* wb; int rs;
  if (u < 30) { wb = (const char*)(wqkv + u * 64); rs = 3 * DM * 2; }
  else if (u < 54) { wb = (const char*)(wg + (u - 30) * 64); rs = FF * 2; }
  else { wb = (const char*)(wu + (u - 54) * 64); rs = FF * 2; }
  const char* ca[4];
#pragma unroll
  for (int i = 0; i < 4; ++i) {
    int kr = part * 214 + (wid + 8 * i) * 8 + (lane >> 3);
    kr = kr > DM - 1 ? DM - 1 : kr;
    ca[i] = wb + (size_t)kr * rs + (lane & 7) * 16;
  }
  L.r0 = *(const u32x4*)a0; L.r1 = *(const u32x4*)a1;
  L.c0 = *(const u32x4*)ca[0]; L.c1 = *(const u32x4*)ca[1]; L.c2 = *(const u32x4*)ca[2]; L.c3 = *(const u32x4*)ca[3];
}
DI void p0_compute(StatLoads& L, char* ws, u64 tag, lptr red) {
  const int lane = threadIdx.x & 63, wid = threadIdx.x >> 6, b = blockIdx.x;
  const unsigned ONES = 0x3f803f80u;
  float s = 0.f, q = 0.f;
#pragma unroll
  for (int e = 0; e < 4; ++e) {
    unsigned a = L.r0[e], c = lane < 16 ? L.r1[e] : 0u;
    s = dot2(a, ONES, s); q = dot2(a, a, q);
    s = dot2(c, ONES, s); q = dot2(c, c, q);
  }
  s = add_ror<1>(s); q = add_ror<1>(q); s = add_ror<2>(s); q = add_ror<2>(q);
  s = add_ror<4>(s); q = add_ror<4>(q); s = add_ror<8>(s); q = add_ror<8>(q);
  s = add_x16(s); q = add_x16(q); s = add_x32(s); q = add_x32(q);
  {
    const float inv = 1.0f / DM;
    float m = s * inv;
    float v = fmaxf(q * inv - m * m, 0.f);
    float r = rsqrtf(v + 1e-5f);
    if (lane == 0) {
      u32x4 g = {__float_as_uint(m), __float_as_uint(r), (unsigned)tag, (unsigned)(tag >> 32)};
      st16_dev(ws + WS_ROWST + (size_t)(8 * b + wid) * 16, g);
    }
  }
  int T = b < 234 ? b : 233;
  int part = T % 3;
  int nrows = part < 2 ? 214 : 212;
  float cs[8];
#pragma unroll
  for (int e = 0; e < 8; ++e) cs[e] = 0.f;
#pragma unroll
  for (int i = 0; i < 4; ++i) {
    u32x4 v = i == 0 ? L.c0 : i == 1 ? L.c1 : i == 2 ? L.c2 : L.c3;
    bool ok = (wid + 8 * i) * 8 + (lane >> 3) < nrows;
#pragma unroll
    for (int e = 0; e < 4; ++e) {
      unsigned w = ok ? v[e] : 0u;
      cs[2 * e] += __uint_as_float(w << 16);
      cs[2 * e + 1] += __uint_as_float(w & 0xffff0000u);
    }
  }
#pragma unroll
  for (int e = 0; e < 8; ++e) cs[e] = add_x32(add_x16(add_ror<8>(cs[e])));
  if (lane < 8) {
    *(LAS f32x4*)(red + wid * 256 + lane * 32) = f32x4{cs[0], cs[1], cs[2], cs[3]};
    *(LAS f32x4*)(red + wid * 256 + lane * 32 + 16) = f32x4{cs[4], cs[5], cs[6], cs[7]};
  }
}
DI void p0_publish(char* ws, u64 tag, lptr red) {
  const int tid = threadIdx.x, b = blockIdx.x;
  if (tid < 32 && b < 234) {
    float c0 = 0.f, c1 = 0.f;
#pragma unroll
    for (int w = 0; w < 8; ++w) { f32x2 v = *(LAS f32x2*)(red + w * 256 + tid * 8); c0 += v[0]; c1 += v[1]; }
    int u = b / 3, part = b - u * 3;
    u32x4 g = {__float_as_uint(c0), __float_as_uint(c1), (unsigned)tag, (unsigned)(tag >> 32)};
    st16_dev(ws + WS_COLST + ((size_t)part * 2496 + u * 32 + tid) * 16, g);
  }
}

struct Work {
  int p1type;
  int p1rt, p1cg;
  int pi, hp;
  int p3rt, p3ct;
};
DI Work get_work() {
  Work w;
  const int e = blockIdx.x & 7, idx = blockIdx.x >> 3;
  const int p = e >> 1, half = e & 1;
  if (idx < 12) { w.p1type = 0; w.p1rt = half * 4 + idx / 3; w.p1cg = 3 * p + idx % 3; }
  else {
    int k = idx - 12, rt, c3;
    if (half == 0) { rt = k / 3; c3 = k % 3; }
    else { if (k == 0) { rt = 6; c3 = 2; } else { rt = 7 + (k - 1) / 3; c3 = (k - 1) % 3; } }
    w.p1type = rt <= 12 ? 1 : 2; w.p1rt = rt; w.p1cg = 3 * p + c3;
  }
  w.pi = idx >> 1; w.hp = e * 2 + (idx & 1);
  const int qd = e >> 1;
  const int start = (qd * 110 + 3) / 4, end = ((qd + 1) * 110 + 3) / 4;
  const int li = start + idx;
  w.p3rt = li < end ? li / 5 : -1; w.p3ct = (e & 1) * 5 + li % 5;
  return w;
}

constexpr int P2_PSTRIDE = 3072 + 32;
constexpr int P2_QOFF = 21 * P2_PSTRIDE;
constexpr int P3_STAGE = 40 * 1024;

constexpr int P2_GO = P2_QOFF + 10 * 1024;
DI void p2_stage(const char* ws, int pi, int hp, lptr sm, int w12) {
  const int lane = threadIdx.x & 63;
  const int kp_min = pi >= 2 ? 0 : 2 - pi;
#pragma unroll 1
  for (int pc = w12; pc < 70; pc += 12) {
    if (pc < 60) {
      int pl = pc / 3, kp = pc - pl * 3;
      if (kp >= kp_min) {
        int hh = pl / 10, w10 = pl - hh * 10;
        int which2 = 1 + (w10 >= 5), quad = w10 - (w10 >= 5 ? 5 : 0);
        const char* src = ws + WS_QKV + (size_t)((which2 * 32 + hp * 2 + hh) * 5 + quad) * QKV_PLANE +
                          (size_t)((pi - 2 + kp) * 128) * 8 + lane * 16;
        glds16_dev(src, sm + pl * P2_PSTRIDE + kp * 1024);
      }
    } else {
      int pl = pc - 60;
      const char* src = ws + WS_QKV + (size_t)(hp * 10 + pl) * QKV_PLANE + (size_t)(pi * 128) * 8 + lane * 16;
      glds16_dev(src, sm + P2_QOFF + pl * 1024);
    }
  }
  WAITV(0);
}

template <bool ISQ>
DI void p1_load(const u16* __restrict__ x, const u16* __restrict__ wqkv, const u16* __restrict__ wg,
                const u16* __restrict__ wu, char* __restrict__ ws, int rt, int cg, lptr sm, u64 tag, int lw) {
  constexpr int BM = ISQ ? 256 : 160, BN = ISQ ? 160 : 256;
  constexpr int APC = BM / 8, NA = APC / 4;
  const int lane = threadIdx.x & 63;
  const int row0 = rt * BM;
  unsigned poff[13];
#pragma unroll
  for (int i = 0; i < 13; ++i) {
    int p = lw + 4 * i;
    if (i < NA) {
      int rl = 8 * p + (lane >> 3), pc = lane & 7;
      int c = pc ^ ((rl >> 1) & 7);
      int gr = row0 + rl; gr = gr > SEQ - 1 ? SEQ - 1 : gr;
      poff[i] = (unsigned)(gr * DM + c * 8) * 2u;
    } else {
      int pb = p - APC;
      if (ISQ) {
        int ci = pb * 64 + lane;
        int kr = ci / 20, pc = ci - kr * 20;
        int c = pc ^ (((kr >> 3) & 1) << 1);
        poff[i] = (unsigned)(kr * (3 * DM) + cg * 160 + c * 8) * 2u;
      } else {
        int u = pb >> 3, kb = pb & 7;
        int r = lane >> 3, pc = lane & 7;
        int f = (((r >> 1) & 1) << 1) | ((kb & 1) << 2);
        int c = pc ^ f;
        poff[i] = (unsigned)((kb * 8 + r) * FF + cg * 128 + (u & 1) * 64 + c * 8) * 2u;
      }
    }
  }
  const char* xb = (const char*)x;
  const char* w0 = (const char*)(ISQ ? wqkv : wg);
  const char* w1 = (const char*)(ISQ ? wqkv : wu);
  auto issue = [&](int so) {
#pragma unroll
    for (int i = 0; i < 13; ++i) {
      const char* b = i < NA ? xb : ((i - NA) < 4 ? w0 : w1);
      glds16(b + poff[i], sm + so + (lw + 4 * i) * 1024);
    }
    xb += 128;
    w0 += ISQ ? 64 * 3 * DM * 2 : 64 * FF * 2;
    w1 += ISQ ? 64 * 3 * DM * 2 : 64 * FF * 2;
  };
  issue(0);
  issue(P1_STAGE);
  issue(2 * P1_STAGE);
  WAITV(26);
  BARRIER();
  int so = 0;
#pragma unroll 1
  for (int t = 0; t < 8; ++t) {
    WAITV(13);
    BARRIER();
    if (t < 7) issue(so);
    so = (so == 2 * P1_STAGE) ? 0 : so + P1_STAGE;
  }
  {
    LAS f32x2* rowstat = (LAS f32x2*)(sm + P1_STATS);
    LAS float* colsum = (LAS float*)(sm + P1_STATS + 2048);
    const int lt = lw * 64 + lane;
    int gr = row0 + (lt < BM ? lt : 0); gr = gr > SEQ - 1 ? SEQ - 1 : gr;
    const int lc = lt < BN / 2 ? lt : 0;
    const int gc = ISQ ? cg * 80 + lc : (lc < 64 ? 960 + cg * 64 + lc : 1728 + cg * 64 + (lc - 64));
    const char* g0 = ws + WS_ROWST + (size_t)gr * 16;
    const char* g1 = ws + WS_COLST + (size_t)gc * 16;
    const char* g2 = g1 + 2496 * 16;
    const char* g3 = g2 + 2496 * 16;
    u32x4 v0, v1, v2, v3;
    for (unsigned spins = 0; spins < SPIN_MAX; ++spins) {
      asm volatile("global_load_dwordx4 %0, %4, off sc1\n\tglobal_load_dwordx4 %1, %5, off sc1\n\t"
                   "global_load_dwordx4 %2, %6, off sc1\n\tglobal_load_dwordx4 %3, %7, off sc1\n\ts_waitcnt vmcnt(0)"
                   : "=&v"(v0), "=&v"(v1), "=&v"(v2), "=&v"(v3) : "v"(g0), "v"(g1), "v"(g2), "v"(g3) : "memory");
      const unsigned tl = (unsigned)tag, th = (unsigned)(tag >> 32);
      bool ok = v0[2] == tl && v0[3] == th && v1[2] == tl && v1[3] == th && v2[2] == tl && v2[3] == th && v3[2] == tl && v3[3] == th;
      if (__all(ok)) break;
      __builtin_amdgcn_s_sleep(1);
    }
    if (lt < BM) rowstat[lt] = f32x2{__uint_as_float(v0[0]), __uint_as_float(v0[1])};
    if (lt < BN / 2)
      *(LAS f32x2*)(colsum + 2 * lt) = f32x2{__uint_as_float(v1[0]) + __uint_as_float(v2[0]) + __uint_as_float(v3[0]),
                                             __uint_as_float(v1[1]) + __uint_as_float(v2[1]) + __uint_as_float(v3[1])};
  }
  BARRIER();
  BARRIER();
}

constexpr int P3_SLOT = 16 * 1024;
constexpr int P3_HGO = P2_GO + 4;
constexpr int P3_OGO = 9 * 16 * 1024;
DI constexpr int p3_slot(int st) { return st < 10 ? (st + 5) % 10 : (st < 14 ? st - 5 : st - 14); }
DI void p3_issue_b(const unsigned* boff, const u16* wo, const u16* wd, int st, lptr sm, int lw) {
  const char* bb = st < 12 ? (const char*)wd + (size_t)st * 128 * DM * 2 : (const char*)wo + (size_t)(st - 12) * 128 * DM * 2;
  lptr d = sm + p3_slot(st) * P3_SLOT;
#pragma unroll
  for (int i = 0; i < 4; ++i) glds16(bb + boff[i], d + (lw + 4 * i) * 1024);
}

DI void loader_main(const u16* x, const u16* wqkv, const u16* wo, const u16* wg, const u16* wu, const u16* wd,
                    char* ws, lptr sm, u64 tag, int lw) {
  const Work W = get_work();
  const int lane = threadIdx.x & 63;
  if (W.p1type == 0) p1_load<true>(x, wqkv, wg, wu, ws, W.p1rt, W.p1cg, sm, tag, lw);
  else if (W.p1type == 1) p1_load<false>(x, wqkv, wg, wu, ws, W.p1rt, W.p1cg, sm, tag, lw);
  else BARRIER();
  const bool has3 = W.p3rt >= 0;
  unsigned boff[4];
#pragma unroll
  for (int i = 0; i < 4; ++i) {
    int kb = lw + 4 * i;
    int r = lane >> 3, pc = lane & 7;
    int f = (((r >> 1) & 1) << 1) | ((kb & 1) << 2);
    boff[i] = (unsigned)((kb * 8 + r) * DM + W.p3ct * 64 + (pc ^ f) * 8) * 2u;
  }
  BARRIER();
  const int pi = W.pi, hp = W.hp;
  {
    int which = lane >> 1, rsel = lane & 1;
    int rtile = rsel == 0 ? (pi >> 1) : (pi >= 2 ? (pi - 2) >> 1 : 0);
    wave_wait_flags(ws + WS_FLAGQ + rtile * 128 + (which * 4 + (hp >> 2)) * 8, lane < 6, tag);
    if (lw == 0 && lane == 0) *(volatile LAS unsigned*)(sm + P2_GO) = 1u;
    p2_stage(ws, pi, hp, sm, 8 + lw);
  }
  BARRIER();
  const int r_lo = W.p3rt * 96, r_hi = (r_lo + 95 > SEQ - 1) ? SEQ - 1 : r_lo + 95;
  if (has3 && lw == 0) {
    int sel = lane >> 4, cgx = lane & 15;
    int gt = sel == 0 ? r_lo / 160 : r_hi / 160;
    wave_wait_flags(ws + WS_FLAGG + gt * 128 + cgx * 8, lane < 32 && cgx < 12, tag);
    if (lane == 0) *(volatile LAS unsigned*)(sm + P3_HGO) = 1u;
  }
  u32x4 breg[8][4];
  if (has3) {
#pragma unroll 1
    for (int st = 0; st < 5; ++st) p3_issue_b(boff, wo, wd, st, sm, lw);
  }
#pragma unroll
  for (int u = 0; u < 8; ++u) {
    const int st = 5 + u;
    const char* bb = st < 12 ? (const char*)wd + (size_t)st * 128 * DM * 2 : (const char*)wo;
#pragma unroll
    for (int i = 0; i < 4; ++i) breg[u][i] = *(const u32x4*)(bb + boff[i]);
  }
#pragma unroll
  for (int u = 0; u < 8; ++u) asm volatile("" : "+v"(breg[u][0]), "+v"(breg[u][1]), "+v"(breg[u][2]), "+v"(breg[u][3]));
  WAITV(0);
  BARRIER();
  if (!has3) return;
  auto put = [&](int u, int st) {
    lptr d = sm + p3_slot(st) * P3_SLOT + lane * 16;
#pragma unroll
    for (int i = 0; i < 4; ++i) *(LAS u32x4*)(d + (lw + 4 * i) * 1024) = breg[u][i];
  };
  put(0, 5); put(1, 6); put(2, 7); put(3, 8); put(4, 9);
#pragma unroll
  for (int u = 0; u < 4; ++u) {
    const char* bb = (const char*)wo + (size_t)(1 + u) * 128 * DM * 2;
#pragma unroll
    for (int i = 0; i < 4; ++i) breg[u][i] = *(const u32x4*)(bb + boff[i]);
  }
#pragma unroll
  for (int u = 0; u < 4; ++u) asm volatile("" : "+v"(breg[u][0]), "+v"(breg[u][1]), "+v"(breg[u][2]), "+v"(breg[u][3]));
  BARRIER();
  put(5, 10); put(6, 11); put(7, 12); put(0, 13);
  if (lw == 0) {
    int sel = lane >> 4, hpx = lane & 15;
    int pn = sel == 0 ? r_lo >> 7 : r_hi >> 7;
    wave_wait_flags(ws + WS_FLAGO + pn * 128 + hpx * 8, lane < 32, tag);
    if (lane == 0) *(volatile LAS u64*)(sm + P3_OGO) = tag;
  }
  BARRIER();
  put(1, 14); put(2, 15); put(3, 16);
  BARRIER();
}

template <bool ISQ>
DI void p1_compute(const u16* __restrict__ x, const u16* __restrict__ wqkv, const u16* __restrict__ wg,
                   const u16* __restrict__ wu, char* __restrict__ ws, int rt, int cg, lptr sm, u64 tag) {
  constexpr int BM = ISQ ? 256 : 160;
  constexpr int MT = ISQ ? 4 : 5, NT = ISQ ? 5 : 4;
  constexpr int NWN = ISQ ? 2 : 4;
  constexpr int ABYTES = BM * 128;
  const int tid = threadIdx.x, lane = tid & 63;
  const int wid = __builtin_amdgcn_readfirstlane(tid >> 6);
  const int wm = wid / NWN, wn = wid % NWN;
  const int row0 = rt * BM;
  {
    StatLoads SL;
    p0_issue(x, wqkv, wg, wu, SL);
    p0_compute(SL, ws, tag, sm + P1_STATS);
  }
  const int r = lane & 15, g = lane >> 4, q = (lane & 15) >> 2, pp = lane & 3;
  unsigned xa0 = (unsigned)(size_t)sm + (unsigned)((wm * MT * 16 + r) * 128 + ((g ^ (r >> 1)) * 16));
  unsigned xa1 = xa0 ^ 64u;
  unsigned wa[2];
  if (ISQ) {
#pragma unroll
    for (int j = 0; j < 2; ++j) {
      int nt = 5 * wn + j;
      wa[j] = (unsigned)(size_t)sm + (unsigned)(ABYTES + (8 * g + q) * 320 + ((nt ^ (g & 1)) * 32) + pp * 8);
    }
  } else {
#pragma unroll
    for (int j = 0; j < 2; ++j) {
      int u = wn >> 1;
      int nt = 2 * (wn & 1) + j;
      int f2 = (q >> 1) | ((g & 1) << 1);
      wa[j] = (unsigned)(size_t)sm + (unsigned)(ABYTES + (u * 8 + g) * 1024 + q * 128 + ((nt ^ f2) * 32) + pp * 8);
    }
  }
  constexpr int W_T = ISQ ? 4 * 320 : 512;
  constexpr int W_S = ISQ ? 32 * 320 : 4096;
  constexpr int W_J = ISQ ? 64 : 16384;

  f32x4 acc[MT][NT];
#pragma unroll
  for (int i = 0; i < MT; ++i)
#pragma unroll
    for (int j = 0; j < NT; ++j) acc[i][j] = f32x4{0.f, 0.f, 0.f, 0.f};

  u32x4 xA[MT], xB[MT];
  u32x2 wAl[NT], wAh[NT], wBl[NT], wBh[NT];
  constexpr int NRD = 2 * NT + MT;
  auto rd = [&](int ri, u32x4* xn, u32x2* wl, u32x2* wh, unsigned xad, int s) {
    if (ri == 2) { xn[0] = lds_u128(lp(xad)); return; }
    int k = ri < 2 ? ri : ri - 1;
    if (k < 2 * NT) {
      int j = k >> 1;
      u32x2 v = lds_tru(lp(wa[j & 1]) + (j >> 1) * W_J + s * W_S + (k & 1) * W_T);
      if (k & 1) wh[j] = v; else wl[j] = v;
    } else {
      int i = k - 2 * NT + 1;
      xn[i] = lds_u128(lp(xad) + i * 2048);
    }
  };
  auto kstep = [&](const u32x4* xc, const u32x2* wcl, const u32x2* wch, u32x4* xn, u32x2* wnl, u32x2* wnh,
                   unsigned xad, int sn) {
#pragma unroll
    for (int k = 0; k < 20; ++k) {
      const int i = k / NT, j = k % NT;
      acc[i][j] = __builtin_amdgcn_mfma_f32_16x16x32_bf16(cat_u(wcl[j], wch[j]), __builtin_bit_cast(bf16x8, xc[i]), acc[i][j], 0, 0, 0);
      __builtin_amdgcn_sched_barrier(0);
      if (k < NRD) rd(k, xn, wnl, wnh, xad, sn);
      __builtin_amdgcn_sched_barrier(0);
    }
  };

  BARRIER();
  STAMP(ws, 1);
  p0_publish(ws, tag, sm + P1_STATS);
#pragma unroll
  for (int ri = 0; ri < NRD; ++ri) rd(ri, xA, wAl, wAh, xa0, 0);
  int so_cur = 0;
#pragma unroll 1
  for (int t = 0; t < 10; ++t) {
    const int so_n = (so_cur == 2 * P1_STAGE) ? 0 : so_cur + P1_STAGE;
    const int dlt = so_n - so_cur;
    kstep(xA, wAl, wAh, xB, wBl, wBh, xa1, 1);
    xa0 += dlt; xa1 += dlt; wa[0] += dlt; wa[1] += dlt;
    BARRIER();
    STAMP(ws, 2 + t);
    kstep(xB, wBl, wBh, xA, wAl, wAh, xa0, 0);
    so_cur = so_n;
  }
  STAMP(ws, 12);

  LAS f32x2* rowstat = (LAS f32x2*)(sm + P1_STATS);
  LAS float* colsum = (LAS float*)(sm + P1_STATS + 2048);
  f32x4 csv[NT];
#pragma unroll
  for (int j = 0; j < NT; ++j) {
    int nb = ISQ ? 5 * wn + j : (j >> 1) * 8 + 2 * wn + (j & 1);
    csv[j] = *(LAS f32x4*)(colsum + nb * 16 + 4 * g);
  }
#pragma unroll
  for (int i = 0; i < MT; ++i) {
    const int ml = wm * MT * 16 + i * 16 + r;
    const f32x2 st = rowstat[ml];
    const int m = row0 + ml;
    if (ISQ) {
      const int which = cg >> 2;
      const float rs = which == 0 ? st[1] * (0.22360679774997896f * 1.4426950408889634f) : st[1];
#pragma unroll
      for (int j = 0; j < NT; ++j) {
        int col = (5 * wn + j) * 16 + 4 * g;
        int hl = col / 20, quad = (col - hl * 20) >> 2;
        int head = (cg & 3) * 8 + hl;
        f32x4 y;
#pragma unroll
        for (int e = 0; e < 4; ++e) y[e] = rs * (acc[i][j][e] - st[0] * csv[j][e]);
        u32x2 o2 = {pack2(y[0], y[1]), pack2(y[2], y[3])};
        st8_dev(ws + WS_QKV + (size_t)((which * 32 + head) * 5 + quad) * QKV_PLANE + (size_t)m * 8, o2);
      }
    } else {
      if (m < SEQ) {
#pragma unroll
        for (int tp = 0; tp < 2; ++tp) {
          float hv[4];
#pragma unroll
          for (int e = 0; e < 4; ++e) {
            float gt = st[1] * (acc[i][tp][e] - st[0] * csv[tp][e]);
            float up = st[1] * (acc[i][tp + 2][e] - st[0] * csv[tp + 2][e]);
            float sg = __builtin_amdgcn_rcpf(1.0f + __expf(-gt));
            hv[e] = gt * sg * up;
          }
          u32x2 o2 = {pack2(hv[0], hv[1]), pack2(hv[2], hv[3])};
          int ks = 20 + 4 * cg + wn;
          int gg = 2 * tp + (g >> 1);
          st8_dev(ws + WS_A2 + ((size_t)(m >> 4) * KS2 + ks) * 1024 + (gg * 16 + r) * 16 + (g & 1) * 8, o2);
        }
      }
    }
  }
  if (tid == 0) { *(volatile LAS unsigned*)(sm + P2_GO) = 0u; *(volatile LAS unsigned*)(sm + P2_GO + 4) = 0u; }
  STAMP(ws, 13);
  WAITV(0);
  BARRIER();
  if (tid == 0)
    __hip_atomic_store((u64*)(ws + (ISQ ? WS_FLAGQ : WS_FLAGG) + rt * 128 + cg * 8), tag, __ATOMIC_RELAXED, __HIP_MEMORY_SCOPE_AGENT);
  STAMP(ws, 14);
}

template <typename F>
DI void p2_compute(char* __restrict__ ws, int pi, int hp, lptr sm, u64 tag, F&& before_store) {
  const int tid = threadIdx.x, lane = tid & 63;
  const int wid = __builtin_amdgcn_readfirstlane(tid >> 6);
  if (tid < 384) *(LAS u32x2*)(sm + 20 * P2_PSTRIDE + tid * 8) = u32x2{0x00003f80u, 0u};
  for (unsigned spins = 0; spins < (SPIN_MAX << 6); ++spins) {
    if (*(volatile LAS unsigned*)(sm + P2_GO) != 0u) break;
    __builtin_amdgcn_s_sleep(2);
  }
  asm volatile("" ::: "memory");
  STAMP(ws, 15);
  p2_stage(ws, pi, hp, sm, wid);
  STAMP(ws, 16);
  BARRIER();
  STAMP(ws, 17);
  const int qt = wid & 3, hh = wid >> 2;
  const int head = hp * 2 + hh;
  const int q0 = pi * 128 + qt * 32;
  const int c = lane & 31, h = lane >> 5;
  bf16x8 qf0; s16x4 qf1;
  {
    lptr qb = sm + P2_QOFF + hh * 5 * 1024 + (qt * 32 + c) * 8;
    u32x2 a = *(LAS u32x2*)(qb + (2 * h) * 1024);
    u32x2 b = *(LAS u32x2*)(qb + (2 * h + 1) * 1024);
    u32x2 d = *(LAS u32x2*)(qb + 4 * 1024);
    u32x4 v0 = {a[0], a[1], b[0], b[1]};
    u32x2 v1 = {h ? 0u : d[0], h ? 0u : d[1]};
    qf0 = __builtin_bit_cast(bf16x8, v0);
    qf1 = __builtin_bit_cast(s16x4, v1);
  }
  const int kt_min = q0 >= 256 ? 0 : (256 - q0) >> 5;
  lptr kb = sm + (hh * 10) * P2_PSTRIDE + (qt * 32 + c) * 8;
  lptr vb;
  {
    const int blk = (lane >> 4) & 1, q = (lane & 15) >> 2, pp = lane & 3;
    int pl = blk ? (pp == 0 ? hh * 10 + 9 : 20) : hh * 10 + 5 + pp;
    vb = sm + pl * P2_PSTRIDE + (qt * 32 + 4 * h + q) * 8;
  }
  u32x4 kf0[9]; u32x2 kf1[9];
#pragma unroll
  for (int kt = 0; kt < 9; ++kt) {
    u32x2 a = *(LAS u32x2*)(kb + (2 * h) * P2_PSTRIDE + kt * 256);
    u32x2 b = *(LAS u32x2*)(kb + (2 * h + 1) * P2_PSTRIDE + kt * 256);
    kf1[kt] = *(LAS u32x2*)(kb + 4 * P2_PSTRIDE + kt * 256);
    kf0[kt] = u32x4{a[0], a[1], b[0], b[1]};
  }
  auto scores = [&](int kt, const f32x16& cin) {
    f32x16 z = __builtin_amdgcn_mfma_f32_32x32x16_bf16(__builtin_bit_cast(bf16x8, kf0[kt]), qf0, cin, 0, 0, 0);
    return __builtin_amdgcn_mfma_f32_32x32x8bf16_1k(__builtin_bit_cast(s16x4, kf1[kt]), qf1, z, 0, 0, 0);
  };
  f32x16 zero16;
#pragma unroll
  for (int e = 0; e < 16; ++e) zero16[e] = 0.f;
  f32x16 O = zero16;
  auto attend = [&](auto FULLC) {
    constexpr bool FULL = decltype(FULLC)::value;
    const bool has0 = FULL || kt_min == 0;
    float mx;
    {
      f32x16 s8 = scores(8, zero16);
      if (has0) {
        f32x16 s0 = scores(0, zero16);
#pragma unroll
        for (int e = 0; e < 16; ++e) { int rr = (e & 3) + 8 * (e >> 2) + 4 * h; s8[e] = rr <= c ? s8[e] : s0[e]; }
      } else {
#pragma unroll
        for (int e = 0; e < 16; ++e) { int rr = (e & 3) + 8 * (e >> 2) + 4 * h; s8[e] = rr <= c ? s8[e] : -INFINITY; }
      }
      float m0 = fmaxf(s8[0], s8[1]), m1 = fmaxf(s8[2], s8[3]);
#pragma unroll
      for (int e = 4; e < 16; e += 4) { m0 = fmaxf(fmaxf(m0, s8[e]), s8[e + 1]); m1 = fmaxf(fmaxf(m1, s8[e + 2]), s8[e + 3]); }
#pragma unroll
      for (int kt = 1; kt < 8; ++kt) {
        if (FULL || kt >= kt_min) {
          f32x16 sv = scores(kt, zero16);
#pragma unroll
          for (int e = 0; e < 16; e += 4) { m0 = fmaxf(fmaxf(m0, sv[e]), sv[e + 1]); m1 = fmaxf(fmaxf(m1, sv[e + 2]), sv[e + 3]); }
        }
      }
      mx = max_x32(fmaxf(m0, m1));
    }
    STAMP(ws, 30);
    f32x16 nm16;
#pragma unroll
    for (int e = 0; e < 16; ++e) nm16[e] = -mx;
    auto pv = [&](int kt, const u32x4& p0, const u32x4& p1) {
      u32x2 lo = lds_tru(vb + kt * 256), hi = lds_tru(vb + kt * 256 + 64);
      u32x2 lo2 = lds_tru(vb + kt * 256 + 128), hi2 = lds_tru(vb + kt * 256 + 192);
      O = __builtin_amdgcn_mfma_f32_32x32x16_bf16(cat_u(lo, hi), __builtin_bit_cast(bf16x8, p0), O, 0, 0, 0);
      O = __builtin_amdgcn_mfma_f32_32x32x16_bf16(cat_u(lo2, hi2), __builtin_bit_cast(bf16x8, p1), O, 0, 0, 0);
    };
    {
      f32x16 s8 = scores(8, nm16);
      if (has0) {
        f32x16 s0 = scores(0, nm16);
#pragma unroll
        for (int e = 0; e < 16; ++e) { int rr = (e & 3) + 8 * (e >> 2) + 4 * h; s8[e] = rr <= c ? s8[e] : s0[e]; }
      }
      u32x4 pa, pb, qa, qb2;
#pragma unroll
      for (int e = 0; e < 8; ++e) {
        unsigned pk = pack2(__builtin_amdgcn_exp2f(s8[2 * e]), __builtin_amdgcn_exp2f(s8[2 * e + 1]));
        int r0 = ((2 * e) & 3) + 8 * ((2 * e) >> 2) + 4 * h;
        unsigned msk = (r0 <= c ? 0xffffu : 0u) | (r0 + 1 <= c ? 0xffff0000u : 0u);
        if (e < 4) { pa[e] = pk & msk; qa[e] = pk & ~msk; } else { pb[e - 4] = pk & msk; qb2[e - 4] = pk & ~msk; }
      }
      pv(8, pa, pb);
      if (has0) pv(0, qa, qb2);
    }
#pragma unroll
    for (int kt = 1; kt < 8; ++kt) {
      if (FULL || kt >= kt_min) {
        f32x16 sv = scores(kt, nm16);
        u32x4 pa, pb;
#pragma unroll
        for (int e = 0; e < 4; ++e) {
          pa[e] = pack2(__builtin_amdgcn_exp2f(sv[2 * e]), __builtin_amdgcn_exp2f(sv[2 * e + 1]));
          pb[e] = pack2(__builtin_amdgcn_exp2f(sv[8 + 2 * e]), __builtin_amdgcn_exp2f(sv[8 + 2 * e + 1]));
        }
        pv(kt, pa, pb);
      }
    }
  };
  if (kt_min == 0) attend(std::true_type{}); else attend(std::false_type{});
  STAMP(ws, 31);
  float l = __shfl(O[8], c + 32);
  float inv = 1.0f / l;
  {
    const int m = q0 + c;
    char* ob = ws + WS_A2 + (size_t)(m >> 4) * KS2 * 1024 + (m & 15) * 16;
#pragma unroll
    for (int grp = 0; grp < 3; ++grp) {
      if (grp < 2 || h == 0) {
        int kidx = head * 20 + 8 * grp + 4 * h;
        u32x2 o2 = {pack2(O[4 * grp] * inv, O[4 * grp + 1] * inv), pack2(O[4 * grp + 2] * inv, O[4 * grp + 3] * inv)};
        st8_dev(ob + (kidx >> 5) * 1024 + ((kidx >> 3) & 3) * 256 + ((kidx >> 2) & 1) * 8, o2);
      }
    }
  }
  STAMP(ws, 32);
  if (before_store()) WAITV(9); else WAITV(0);
  STAMP(ws, 33);
  BARRIER();
  if (tid == 0)
    __hip_atomic_store((u64*)(ws + WS_FLAGO + pi * 128 + hp * 8), tag, __ATOMIC_RELAXED, __HIP_MEMORY_SCOPE_AGENT);
  STAMP(ws, 18);
}

struct P3A { u32x4 f[3][3]; };
DI void p3_load_a(u32x4* dst, __amdgpu_buffer_rsrc_t rs, const unsigned* aoff, int st) {
  const int ks0 = st < 12 ? 20 + 4 * st : 4 * (st - 12);
#pragma unroll
  for (int i = 0; i < 3; ++i) dst[i] = __builtin_amdgcn_raw_buffer_load_b128(rs, aoff[i], ks0 * 1024, 16);
}
DI void p3_a_setup(unsigned* aoff, int rt) {
  const int lane = threadIdx.x & 63, wid = threadIdx.x >> 6;
#pragma unroll
  for (int i = 0; i < 3; ++i) {
    int gmt = rt * 6 + (wid >> 2) * 3 + i; gmt = gmt > 127 ? 127 : gmt;
    aoff[i] = (unsigned)((gmt * KS2 + (wid & 3)) * 1024 + lane * 16);
  }
}

DI void p3_compute(const u16* __restrict__ x, char* __restrict__ ws, u16* __restrict__ out, int rt, int ct, lptr sm,
                   P3A& A, __amdgpu_buffer_rsrc_t rs, const unsigned* aoff, u64 tag) {
  const int tid = threadIdx.x, lane = tid & 63;
  const int wid = __builtin_amdgcn_readfirstlane(tid >> 6);
  const int ks4 = wid & 3, mh = wid >> 2;
  const int r = lane & 15, g = lane >> 4, q = (lane & 15) >> 2, pp = lane & 3;
  unsigned wa[4];
#pragma unroll
  for (int j = 0; j < 4; ++j) {
    int f2 = (q >> 1) | ((g & 1) << 1);
    wa[j] = (unsigned)(size_t)sm + (unsigned)((ks4 * 4 + g) * 1024 + q * 128 + ((j ^ f2) * 32) + pp * 8);
  }
  f32x4 acc[3][4];
#pragma unroll
  for (int i = 0; i < 3; ++i)
#pragma unroll
    for (int j = 0; j < 4; ++j) acc[i][j] = f32x4{0.f, 0.f, 0.f, 0.f};
  u32x4 xres[2];
#pragma unroll
  for (int t = 0; t < 17; ++t) {
    if (t == 5 || t == 10 || t == 14) BARRIER();
    if ((t & 3) == 0) STAMP(ws, 21 + t / 4);
    const int so = p3_slot(t) * P3_SLOT;
    u32x2 wl[4], wh[4];
#pragma unroll
    for (int j = 0; j < 4; ++j) {
      wl[j] = lds_tru(lp(wa[j] + so));
      wh[j] = lds_tru(lp(wa[j] + so) + 512);
    }
    u32x4 xc[3];
#pragma unroll
    for (int i = 0; i < 3; ++i) xc[i] = A.f[t % 3][i];
    asm volatile("" : "+v"(xc[0]), "+v"(xc[1]), "+v"(xc[2]));
    if (t == 9) {
      for (unsigned spins = 0; spins < (SPIN_MAX << 6); ++spins) {
        if (*(volatile LAS u64*)(sm + P3_OGO) == tag) break;
        __builtin_amdgcn_s_sleep(2);
      }
      asm volatile("" ::: "memory");
    }
    if (t + 3 < 17) p3_load_a(A.f[t % 3], rs, aoff, t + 3);
    if (t == 14) {
#pragma unroll
      for (int u = 0; u < 2; ++u) {
        int it = tid + 512 * u; it = it < 768 ? it : 0;
        int m = rt * 96 + (it >> 3); m = m > SEQ - 1 ? SEQ - 1 : m;
        xres[u] = *(const u32x4*)(x + (size_t)m * DM + ct * 64 + (it & 7) * 8);
      }
    }
#pragma unroll
    for (int i = 0; i < 3; ++i)
#pragma unroll
      for (int j = 0; j < 4; ++j)
        acc[i][j] = __builtin_amdgcn_mfma_f32_16x16x32_bf16(cat_u(wl[j], wh[j]), __builtin_bit_cast(bf16x8, xc[i]), acc[i][j], 0, 0, 0);
  }
  asm volatile("" : "+v"(xres[0]), "+v"(xres[1]));
  BARRIER();
  STAMP(ws, 26);
  constexpr int RS = 68;
  {
    LAS float* red = (LAS float*)sm + ks4 * 96 * RS;
#pragma unroll
    for (int i = 0; i < 3; ++i)
#pragma unroll
      for (int j = 0; j < 4; ++j)
        *(LAS f32x4*)(red + ((mh * 3 + i) * 16 + r) * RS + j * 16 + 4 * g) = acc[i][j];
  }
  BARRIER();
#pragma unroll
  for (int u = 0; u < 2; ++u) {
    const int it = tid + 512 * u;
    int ml = it >> 3, c8 = it & 7;
    int m = rt * 96 + ml;
    if (it < 768 && m < SEQ) {
      LAS float* r0 = (LAS float*)sm + ml * RS + c8 * 8;
      f32x4 a0 = *(LAS f32x4*)r0, a1 = *(LAS f32x4*)(r0 + 4);
#pragma unroll
      for (int k = 1; k < 4; ++k) { a0 += *(LAS f32x4*)(r0 + k * 96 * RS); a1 += *(LAS f32x4*)(r0 + k * 96 * RS + 4); }
      const u32x4 xv = xres[u];
      u32x4 o;
#pragma unroll
      for (int e = 0; e < 4; ++e) {
        float lo = __uint_as_float(xv[e] << 16), hi = __uint_as_float(xv[e] & 0xffff0000u);
        float va = e < 2 ? a0[2 * e] : a1[2 * e - 4], vb2 = e < 2 ? a0[2 * e + 1] : a1[2 * e - 3];
        o[e] = pack2(lo + va, hi + vb2);
      }
      *(u32x4*)(out + (size_t)m * DM + ct * 64 + c8 * 8) = o;
    }
  }
  STAMP(ws, 27);
}

__global__ __launch_bounds__(768, 3) void k_fused(const u16* __restrict__ x, const u16* __restrict__ wqkv,
                                                  const u16* __restrict__ wo, const u16* __restrict__ wg,
                                                  const u16* __restrict__ wu, const u16* __restrict__ wd,
                                                  char* __restrict__ ws, u16* __restrict__ out) {
  extern __shared__ __attribute__((aligned(16))) char smem[];
  lptr sm = (lptr)smem;
  u64 tag = hw_dispatch_id() * 0x9E3779B97F4A7C15ull + (u64)(size_t)__builtin_amdgcn_dispatch_ptr() * 0xC2B2AE3D27D4EB4Full;
  tag ^= tag >> 29;
  tag = (tag | 1ull) & ~0x100ull;
  STAMP(ws, 0);
  const int wid = __builtin_amdgcn_readfirstlane(threadIdx.x >> 6);
  if (wid >= 8) {
    loader_main(x, wqkv, wo, wg, wu, wd, ws, sm, tag, wid - 8);
    return;
  }
  const Work W = get_work();
  if (W.p1type == 0) p1_compute<true>(x, wqkv, wg, wu, ws, W.p1rt, W.p1cg, sm, tag);
  else if (W.p1type == 1) p1_compute<false>(x, wqkv, wg, wu, ws, W.p1rt, W.p1cg, sm, tag);
  else {
    StatLoads SL;
    p0_issue(x, wqkv, wg, wu, SL);
    p0_compute(SL, ws, tag, sm + P1_STATS);
    BARRIER();
    p0_publish(ws, tag, sm + P1_STATS);
    if (threadIdx.x == 0) { *(volatile LAS unsigned*)(sm + P2_GO) = 0u; *(volatile LAS unsigned*)(sm + P2_GO + 4) = 0u; }
    BARRIER();
  }
  P3A A3;
  unsigned aoff[3];
  p3_a_setup(aoff, W.p3rt >= 0 ? W.p3rt : 0);
  const __amdgpu_buffer_rsrc_t rs = __builtin_amdgcn_make_buffer_rsrc(ws + WS_A2, 0, 128 * KS2 * 1024, 0x00020000);
  p2_compute(ws, W.pi, W.hp, sm, tag, [&]() {
    if (W.p3rt >= 0) {
      for (unsigned spins = 0; spins < (SPIN_MAX << 6); ++spins) {
        if (*(volatile LAS unsigned*)(sm + P3_HGO) != 0u) break;
        __builtin_amdgcn_s_sleep(2);
      }
      asm volatile("" ::: "memory");
#pragma unroll
      for (int st = 0; st < 3; ++st) p3_load_a(A3.f[st], rs, aoff, st);
      asm volatile("" ::: "memory");
      return true;
    }
    return false;
  });
  if (W.p3rt >= 0) p3_compute(x, ws, out, W.p3rt, W.p3ct, sm, A3, rs, aoff, tag);
}

extern "C" void kernel_launch(void* const* d_in, const int* in_sizes, int n_in,
                              void* d_out, int out_size, void* d_ws, size_t ws_size,
                              hipStream_t stream) {
  const u16* x = (const u16*)d_in[0];
  const u16* wqkv = (const u16*)d_in[1];
  const u16* wo = (const u16*)d_in[2];
  const u16* wg = (const u16*)d_in[4];
  const u16* wu = (const u16*)d_in[5];
  const u16* wd = (const u16*)d_in[6];
  (void)hipFuncSetAttribute((const void*)k_fused, hipFuncAttributeMaxDynamicSharedMemorySize, LDS_TOTAL);
  k_fused<<<256, 768, LDS_TOTAL, stream>>>(x, wqkv, wo, wg, wu, wd, (char*)d_ws, (u16*)d_out);
}
```

```cpp
#include <hip/hip_runtime.h>
#include <hip/hip_bf16.h>
#include <type_traits>

typedef __attribute__((ext_vector_type(8))) __bf16 bf16x8;
typedef __attribute__((ext_vector_type(2))) __bf16 bf16x2;
typedef __attribute__((ext_vector_type(4))) short s16x4;
typedef __attribute__((ext_vector_type(4))) float f32x4;
typedef __attribute__((ext_vector_type(2))) float f32x2;
typedef __attribute__((ext_vector_type(16))) float f32x16;
typedef __attribute__((ext_vector_type(4))) unsigned u32x4;
typedef __attribute__((ext_vector_type(2))) unsigned u32x2;
#define LAS __attribute__((address_space(3)))
typedef LAS char* lptr;
typedef unsigned short u16;
typedef unsigned long long u64;

#define DI __device__ __forceinline__
extern "C" __device__ u64 hw_dispatch_id() __asm("llvm.amdgcn.dispatch.id");

constexpr int SEQ = 2048, DM = 640, FF = 1536;
constexpr int KS2 = 68;
constexpr size_t QKV_PLANE = (size_t)SEQ * 8;
constexpr size_t WS_QKV = 0;
constexpr size_t WS_A2 = WS_QKV + 3 * 32 * 5 * QKV_PLANE;
constexpr size_t WS_ROWST = WS_A2 + (size_t)128 * KS2 * 1024;
constexpr size_t WS_COLST = WS_ROWST + 2048 * 16;
constexpr size_t WS_FLAGQ = WS_COLST + 3 * 2496 * 16;
constexpr size_t WS_FLAGG = WS_FLAGQ + 8 * 128;
constexpr size_t WS_FLAGO = WS_FLAGG + 13 * 128;

DI void glds16(const void* g, lptr l) {
  __builtin_amdgcn_global_load_lds((const __attribute__((address_space(1))) unsigned*)g, (LAS unsigned*)l, 16, 0, 0);
}
DI void glds16_dev(const void* g, lptr l) {
  __builtin_amdgcn_global_load_lds((const __attribute__((address_space(1))) unsigned*)g, (LAS unsigned*)l, 16, 0, 16);
}
DI float dot2(unsigned a, unsigned b, float c) {
  return __builtin_amdgcn_fdot2_f32_bf16(__builtin_bit_cast(bf16x2, a), __builtin_bit_cast(bf16x2, b), c, false);
}
DI unsigned pack2(float a, float b) {
  bf16x2 v; v[0] = (__bf16)a; v[1] = (__bf16)b;
  return __builtin_bit_cast(unsigned, v);
}
#define WAITV(n) asm volatile("s_waitcnt vmcnt(" #n ")" ::: "memory")
#define BARRIER() do { asm volatile("" ::: "memory"); __builtin_amdgcn_s_waitcnt(0xC07F); __builtin_amdgcn_s_barrier(); asm volatile("" ::: "memory"); } while (0)
DI u32x4 lds_u128(lptr p) { return *(LAS u32x4*)p; }
DI u32x2 lds_tru(lptr p) { return __builtin_bit_cast(u32x2, __builtin_amdgcn_ds_read_tr16_b64_v4i16((LAS s16x4*)p)); }
DI bf16x8 cat_u(u32x2 lo, u32x2 hi) { u32x4 v = {lo[0], lo[1], hi[0], hi[1]}; return __builtin_bit_cast(bf16x8, v); }
DI lptr lp(unsigned a) { return (lptr)(size_t)a; }

DI void st8_dev(void* p, u32x2 v) {
  __hip_atomic_store((u64*)p, ((u64)v[1] << 32) | v[0], __ATOMIC_RELAXED, __HIP_MEMORY_SCOPE_AGENT);
}
DI void st16_dev(void* p, u32x4 v) {
  asm volatile("global_store_dwordx4 %0, %1, off sc1\n\ts_nop 1" :: "v"(p), "v"(v) : "memory");
}
DI u64 ld8_dev(const void* p) { return __hip_atomic_load((const u64*)p, __ATOMIC_RELAXED, __HIP_MEMORY_SCOPE_AGENT); }
constexpr unsigned SPIN_MAX = 1u << 16;
DI void wave_wait_flags(const char* p, bool act, u64 tag) {
  for (unsigned spins = 0; spins < SPIN_MAX; ++spins) {
    bool ok = true;
    if (act) ok = (ld8_dev(p) == tag);
    if (__all(ok)) break;
    __builtin_amdgcn_s_sleep(1);
  }
  asm volatile("" ::: "memory");
}

template <int N> DI float add_ror(float v) {
  return v + __builtin_bit_cast(float, __builtin_amdgcn_update_dpp(0, __builtin_bit_cast(int, v), 0x120 + N, 0xf, 0xf, false));
}
DI float add_x16(float v) {
  auto r = __builtin_amdgcn_permlane16_swap(__float_as_uint(v), __float_as_uint(v), false, false);
  return __uint_as_float(r[0]) + __uint_as_float(r[1]);
}
DI float add_x32(float v) {
  auto r = __builtin_amdgcn_permlane32_swap(__float_as_uint(v), __float_as_uint(v), false, false);
  return __uint_as_float(r[0]) + __uint_as_float(r[1]);
}
template <int N> DI float max_ror(float v) {
  return fmaxf(v, __builtin_bit_cast(float, __builtin_amdgcn_update_dpp(0, __builtin_bit_cast(int, v), 0x120 + N, 0xf, 0xf, false)));
}
DI float max_x16(float v) {
  auto r = __builtin_amdgcn_permlane16_swap(__float_as_uint(v), __float_as_uint(v), false, false);
  return fmaxf(__uint_as_float(r[0]), __uint_as_float(r[1]));
}
DI float max_x32(float v) {
  auto r = __builtin_amdgcn_permlane32_swap(__float_as_uint(v), __float_as_uint(v), false, false);
  return fmaxf(__uint_as_float(r[0]), __uint_as_float(r[1]));
}

constexpr int P1_STAGE = 52 * 1024;
constexpr int P1_STATS = 3 * P1_STAGE;
constexpr int LDS_TOTAL = 160 * 1024;

struct StatLoads { u32x4 r0, r1, c0, c1, c2, c3; };

DI void p0_issue(const u16* x, const u16* wqkv, const u16* wg, const u16* wu, StatLoads& L) {
  const int lane = threadIdx.x & 63, wid = threadIdx.x >> 6, b = blockIdx.x;
  const char* rp = (const char*)(x + (size_t)(8 * b + wid) * DM);
  const char* a0 = rp + lane * 16;
  const char* a1 = rp + 1024 + (lane & 15) * 16;
  int T = b < 234 ? b : 233;
  int u = T / 3, part = T - u * 3;
  const char* wb; int rs;
  if (u < 30) { wb = (const char*)(wqkv + u * 64); rs = 3 * DM * 2; }
  else if (u < 54) { wb = (const char*)(wg + (u - 30) * 64); rs = FF * 2; }
  else { wb = (const char*)(wu + (u - 54) * 64); rs = FF * 2; }
  const char* ca[4];
#pragma unroll
  for (int i = 0; i < 4; ++i) {
    int kr = part * 214 + (wid + 8 * i) * 8 + (lane >> 3);
    kr = kr > DM - 1 ? DM - 1 : kr;
    ca[i] = wb + (size_t)kr * rs + (lane & 7) * 16;
  }
  L.r0 = *(const u32x4*)a0; L.r1 = *(const u32x4*)a1;
  L.c0 = *(const u32x4*)ca[0]; L.c1 = *(const u32x4*)ca[1]; L.c2 = *(const u32x4*)ca[2]; L.c3 = *(const u32x4*)ca[3];
}
DI void p0_compute(StatLoads& L, char* ws, u64 tag, lptr red) {
  const int lane = threadIdx.x & 63, wid = threadIdx.x >> 6, b = blockIdx.x;
  const unsigned ONES = 0x3f803f80u;
  float s = 0.f;
#pragma unroll
  for (int e = 0; e < 4; ++e) {
    s = dot2(L.r0[e], ONES, s);
    s = dot2(lane < 16 ? L.r1[e] : 0u, ONES, s);
  }
  s = add_x32(add_x16(add_ror<8>(add_ror<4>(add_ror<2>(add_ror<1>(s))))));
  {
    const float inv = 1.0f / DM;
    const float m = s * inv;
    float q = 0.f, q2 = 0.f;
#pragma unroll
    for (int e = 0; e < 4; ++e) {
      float d0 = __uint_as_float(L.r0[e] << 16) - m, d1 = __uint_as_float(L.r0[e] & 0xffff0000u) - m;
      float d2 = __uint_as_float(L.r1[e] << 16) - m, d3 = __uint_as_float(L.r1[e] & 0xffff0000u) - m;
      q = __builtin_fmaf(d0, d0, q); q = __builtin_fmaf(d1, d1, q);
      q2 = __builtin_fmaf(d2, d2, q2); q2 = __builtin_fmaf(d3, d3, q2);
    }
    q += lane < 16 ? q2 : 0.f;
    q = add_x32(add_x16(add_ror<8>(add_ror<4>(add_ror<2>(add_ror<1>(q))))));
    const float r = rsqrtf(q * inv + 1e-5f);
    if (lane == 0) {
      u32x4 g = {__float_as_uint(m), __float_as_uint(r), (unsigned)tag, (unsigned)(tag >> 32)};
      st16_dev(ws + WS_ROWST + (size_t)(8 * b + wid) * 16, g);
    }
  }
  int T = b < 234 ? b : 233;
  int part = T % 3;
  int nrows = part < 2 ? 214 : 212;
  float cs[8];
#pragma unroll
  for (int e = 0; e < 8; ++e) cs[e] = 0.f;
#pragma unroll
  for (int i = 0; i < 4; ++i) {
    u32x4 v = i == 0 ? L.c0 : i == 1 ? L.c1 : i == 2 ? L.c2 : L.c3;
    bool ok = (wid + 8 * i) * 8 + (lane >> 3) < nrows;
#pragma unroll
    for (int e = 0; e < 4; ++e) {
      unsigned w = ok ? v[e] : 0u;
      cs[2 * e] += __uint_as_float(w << 16);
      cs[2 * e + 1] += __uint_as_float(w & 0xffff0000u);
    }
  }
#pragma unroll
  for (int e = 0; e < 8; ++e) cs[e] = add_x32(add_x16(add_ror<8>(cs[e])));
  if (lane < 8) {
    *(LAS f32x4*)(red + wid * 256 + lane * 32) = f32x4{cs[0], cs[1], cs[2], cs[3]};
    *(LAS f32x4*)(red + wid * 256 + lane * 32 + 16) = f32x4{cs[4], cs[5], cs[6], cs[7]};
  }
}
DI void p0_publish(char* ws, u64 tag, lptr red) {
  const int tid = threadIdx.x, b = blockIdx.x;
  if (tid < 32 && b < 234) {
    float c0 = 0.f, c1 = 0.f;
#pragma unroll
    for (int w = 0; w < 8; ++w) { f32x2 v = *(LAS f32x2*)(red + w * 256 + tid * 8); c0 += v[0]; c1 += v[1]; }
    int u = b / 3, part = b - u * 3;
    u32x4 g = {__float_as_uint(c0), __float_as_uint(c1), (unsigned)tag, (unsigned)(tag >> 32)};
    st16_dev(ws + WS_COLST + ((size_t)part * 2496 + u * 32 + tid) * 16, g);
  }
}


struct Work {
  int p1type;
  int p1rt, p1cg;
  int pi, hp;
  int p3rt, p3ct;
};
DI Work get_work() {
  Work w;
  const int e = blockIdx.x & 7, idx = blockIdx.x >> 3;
  const int p = e >> 1, half = e & 1;
  if (idx < 12) { w.p1type = 0; w.p1rt = half * 4 + idx / 3; w.p1cg = 3 * p + idx % 3; }
  else {
    int k = idx - 12, rt, c3;
    if (half == 0) { rt = k / 3; c3 = k % 3; }
    else { if (k == 0) { rt = 6; c3 = 2; } else { rt = 7 + (k - 1) / 3; c3 = (k - 1) % 3; } }
    w.p1type = rt <= 12 ? 1 : 2; w.p1rt = rt; w.p1cg = 3 * p + c3;
  }
  w.pi = idx >> 1; w.hp = e * 2 + (idx & 1);
  const int qd = e >> 1;
  const int start = (qd * 110 + 3) / 4, end = ((qd + 1) * 110 + 3) / 4;
  const int li = start + idx;
  w.p3rt = li < end ? li / 5 : -1; w.p3ct = (e & 1) * 5 + li % 5;
  return w;
}

constexpr int P2_PSTRIDE = 3072 + 32;
constexpr int P2_QOFF = 21 * P2_PSTRIDE;

constexpr int P2_GO = P2_QOFF + 10 * 1024;
DI void p2_stage(const char* ws, int pi, int hp, lptr sm, int w12) {
  const int lane = threadIdx.x & 63;
  const int kp_min = pi >= 2 ? 0 : 2 - pi;
#pragma unroll 1
  for (int pc = w12; pc < 70; pc += 12) {
    if (pc < 60) {
      int pl = pc / 3, kp = pc - pl * 3;
      if (kp >= kp_min) {
        int hh = pl / 10, w10 = pl - hh * 10;
        int which2 = 1 + (w10 >= 5), quad = w10 - (w10 >= 5 ? 5 : 0);
        const char* src = ws + WS_QKV + (size_t)((which2 * 32 + hp * 2 + hh) * 5 + quad) * QKV_PLANE +
                          (size_t)((pi - 2 + kp) * 128) * 8 + lane * 16;
        glds16_dev(src, sm + pl * P2_PSTRIDE + kp * 1024);
      }
    } else {
      int pl = pc - 60;
      const char* src = ws + WS_QKV + (size_t)(hp * 10 + pl) * QKV_PLANE + (size_t)(pi * 128) * 8 + lane * 16;
      glds16_dev(src, sm + P2_QOFF + pl * 1024);
    }
  }
  WAITV(0);
}

template <bool ISQ, typename BL>
DI void p1_load(const u16* __restrict__ x, const u16* __restrict__ wqkv, const u16* __restrict__ wg,
                const u16* __restrict__ wu, char* __restrict__ ws, int rt, int cg, lptr sm, u64 tag, int lw, BL&& bload) {
  constexpr int BM = ISQ ? 256 : 160, BN = ISQ ? 160 : 256;
  constexpr int APC = BM / 8, NA = APC / 4;
  const int lane = threadIdx.x & 63;
  const int row0 = rt * BM;
  unsigned poff[13];
#pragma unroll
  for (int i = 0; i < 13; ++i) {
    int p = lw + 4 * i;
    if (i < NA) {
      int rl = 8 * p + (lane >> 3), pc = lane & 7;
      int c = pc ^ ((rl >> 1) & 7);
      int gr = row0 + rl; gr = gr > SEQ - 1 ? SEQ - 1 : gr;
      poff[i] = (unsigned)(gr * DM + c * 8) * 2u;
    } else {
      int pb = p - APC;
      if (ISQ) {
        int ci = pb * 64 + lane;
        int kr = ci / 20, pc = ci - kr * 20;
        int c = pc ^ (((kr >> 3) & 1) << 1);
        poff[i] = (unsigned)(kr * (3 * DM) + cg * 160 + c * 8) * 2u;
      } else {
        int u = pb >> 3, kb = pb & 7;
        int r = lane >> 3, pc = lane & 7;
        int f = (((r >> 1) & 1) << 1) | ((kb & 1) << 2);
        int c = pc ^ f;
        poff[i] = (unsigned)((kb * 8 + r) * FF + cg * 128 + (u & 1) * 64 + c * 8) * 2u;
      }
    }
  }
  const char* xb = (const char*)x;
  const char* w0 = (const char*)(ISQ ? wqkv : wg);
  const char* w1 = (const char*)(ISQ ? wqkv : wu);
  auto issue = [&](int so) {
#pragma unroll
    for (int i = 0; i < 13; ++i) {
      const char* b = i < NA ? xb : ((i - NA) < 4 ? w0 : w1);
      glds16(b + poff[i], sm + so + (lw + 4 * i) * 1024);
    }
    xb += 128;
    w0 += ISQ ? 64 * 3 * DM * 2 : 64 * FF * 2;
    w1 += ISQ ? 64 * 3 * DM * 2 : 64 * FF * 2;
  };
  issue(0);
  issue(P1_STAGE);
  WAITV(13);
  BARRIER();
  issue(2 * P1_STAGE);
  bload(0); bload(1);
#pragma unroll
  for (int t = 0; t < 8; ++t) {
    if (t == 0) WAITV(15); else WAITV(17);
    BARRIER();
    if (t < 7) {
      issue((t % 3) * P1_STAGE);
      bload(2 + 2 * t); bload(3 + 2 * t);
    }
  }
#pragma unroll
  for (int i = 16; i < 24; ++i) bload(i);
  {
    LAS f32x2* rowstat = (LAS f32x2*)(sm + P1_STATS);
    LAS float* colsum = (LAS float*)(sm + P1_STATS + 2048);
    const int lt = lw * 64 + lane;
    int gr = row0 + (lt < BM ? lt : 0); gr = gr > SEQ - 1 ? SEQ - 1 : gr;
    const int lc = lt < BN / 2 ? lt : 0;
    const int gc = ISQ ? cg * 80 + lc : (lc < 64 ? 960 + cg * 64 + lc : 1728 + cg * 64 + (lc - 64));
    const char* g0 = ws + WS_ROWST + (size_t)gr * 16;
    const char* g1 = ws + WS_COLST + (size_t)gc * 16;
    const char* g2 = g1 + 2496 * 16;
    const char* g3 = g2 + 2496 * 16;
    u32x4 v0, v1, v2, v3;
    for (unsigned spins = 0; spins < SPIN_MAX; ++spins) {
      asm volatile("global_load_dwordx4 %0, %4, off sc1\n\tglobal_load_dwordx4 %1, %5, off sc1\n\t"
                   "global_load_dwordx4 %2, %6, off sc1\n\tglobal_load_dwordx4 %3, %7, off sc1\n\ts_waitcnt vmcnt(0)"
                   : "=&v"(v0), "=&v"(v1), "=&v"(v2), "=&v"(v3) : "v"(g0), "v"(g1), "v"(g2), "v"(g3) : "memory");
      const unsigned tl = (unsigned)tag, th = (unsigned)(tag >> 32);
      bool ok = v0[2] == tl && v0[3] == th && v1[2] == tl && v1[3] == th && v2[2] == tl && v2[3] == th && v3[2] == tl && v3[3] == th;
      if (__all(ok)) break;
      __builtin_amdgcn_s_sleep(1);
    }
    if (lt < BM) rowstat[lt] = f32x2{__uint_as_float(v0[0]), __uint_as_float(v0[1])};
    if (lt < BN / 2)
      *(LAS f32x2*)(colsum + 2 * lt) = f32x2{__uint_as_float(v1[0]) + __uint_as_float(v2[0]) + __uint_as_float(v3[0]),
                                             __uint_as_float(v1[1]) + __uint_as_float(v2[1]) + __uint_as_float(v3[1])};
  }
  BARRIER();
#pragma unroll
  for (int i = 24; i < 32; ++i) bload(i);
  BARRIER();
}

constexpr int P3_SLOT = 16 * 1024;
constexpr int P3_HGO = P2_GO + 4;
DI constexpr int p3_slot(int st) { return st < 10 ? (st + 5) % 10 : (st < 14 ? st - 5 : st - 14); }
DI void p3_issue_b(const unsigned* boff, const u16* wo, const u16* wd, int st, lptr sm, int lw) {
  const char* bb = st < 12 ? (const char*)wd + (size_t)st * 128 * DM * 2 : (const char*)wo + (size_t)(st - 12) * 128 * DM * 2;
  lptr d = sm + p3_slot(st) * P3_SLOT;
#pragma unroll
  for (int i = 0; i < 4; ++i) glds16(bb + boff[i], d + (lw + 4 * i) * 1024);
}

DI void loader_main(const u16* x, const u16* wqkv, const u16* wo, const u16* wg, const u16* wu, const u16* wd,
                    char* ws, lptr sm, u64 tag, int lw) {
  const Work W = get_work();
  const int lane = threadIdx.x & 63;
  const bool has3 = W.p3rt >= 0;
  unsigned boff[4];
#pragma unroll
  for (int i = 0; i < 4; ++i) {
    int kb = lw + 4 * i;
    int r = lane >> 3, pc = lane & 7;
    int f = (((r >> 1) & 1) << 1) | ((kb & 1) << 2);
    boff[i] = (unsigned)((kb * 8 + r) * DM + W.p3ct * 64 + (pc ^ f) * 8) * 2u;
  }
  u32x4 breg[8][4];
  auto bload = [&](int idx) {
    const int st = 5 + (idx >> 2);
    const char* bb = st < 12 ? (const char*)wd + (size_t)st * 128 * DM * 2 : (const char*)wo;
    asm volatile("global_load_dwordx4 %0, %1, %2" : "=v"(breg[idx >> 2][idx & 3]) : "v"(boff[idx & 3]), "s"(bb) : "memory");
  };
  if (W.p1type == 0) p1_load<true>(x, wqkv, wg, wu, ws, W.p1rt, W.p1cg, sm, tag, lw, bload);
  else if (W.p1type == 1) p1_load<false>(x, wqkv, wg, wu, ws, W.p1rt, W.p1cg, sm, tag, lw, bload);
  else {
#pragma unroll
    for (int i = 0; i < 32; ++i) bload(i);
    BARRIER();
  }
  BARRIER();
  const int pi = W.pi, hp = W.hp;
  {
    int which = lane >> 1, rsel = lane & 1;
    int rtile = rsel == 0 ? (pi >> 1) : (pi >= 2 ? (pi - 2) >> 1 : 0);
    wave_wait_flags(ws + WS_FLAGQ + rtile * 128 + (which * 4 + (hp >> 2)) * 8, lane < 6, tag);
    if (lane == 0) *(volatile LAS unsigned*)(sm + P2_GO) = 1u;
    p2_stage(ws, pi, hp, sm, 8 + lw);
  }
  BARRIER();
  const int r_lo = W.p3rt * 96, r_hi = (r_lo + 95 > SEQ - 1) ? SEQ - 1 : r_lo + 95;
  if (has3 && lw == 0) {
    int sel = lane >> 4, cgx = lane & 15;
    int gt = sel == 0 ? r_lo / 160 : r_hi / 160;
    wave_wait_flags(ws + WS_FLAGG + gt * 128 + cgx * 8, lane < 32 && cgx < 12, tag);
    if (lane == 0) *(volatile LAS unsigned*)(sm + P3_HGO) = 1u;
  }
  if (has3) {
#pragma unroll 1
    for (int st = 0; st < 5; ++st) p3_issue_b(boff, wo, wd, st, sm, lw);
  }
  WAITV(0);
#pragma unroll
  for (int u = 0; u < 8; ++u) asm volatile("" : "+v"(breg[u][0]), "+v"(breg[u][1]), "+v"(breg[u][2]), "+v"(breg[u][3]));
  BARRIER();
  if (!has3) return;
  auto put = [&](int u, int st) {
    lptr d = sm + p3_slot(st) * P3_SLOT + lane * 16;
#pragma unroll
    for (int i = 0; i < 4; ++i) *(LAS u32x4*)(d + (lw + 4 * i) * 1024) = breg[u][i];
  };
  put(0, 5); put(1, 6); put(2, 7); put(3, 8); put(4, 9);
  BARRIER();
#pragma unroll
  for (int u = 0; u < 4; ++u) {
    const char* bb = (const char*)wo + (size_t)(1 + u) * 128 * DM * 2;
#pragma unroll
    for (int i = 0; i < 4; ++i)
      asm volatile("global_load_dwordx4 %0, %1, %2" : "=v"(breg[u][i]) : "v"(boff[i]), "s"(bb) : "memory");
  }
  put(5, 10); put(6, 11); put(7, 12);
  WAITV(0);
#pragma unroll
  for (int u = 0; u < 4; ++u) asm volatile("" : "+v"(breg[u][0]), "+v"(breg[u][1]), "+v"(breg[u][2]), "+v"(breg[u][3]));
  put(0, 13);
  BARRIER();
  put(1, 14); put(2, 15); put(3, 16);
  BARRIER();
}

template <bool ISQ>
DI void p1_compute(const u16* __restrict__ x, const u16* __restrict__ wqkv, const u16* __restrict__ wg,
                   const u16* __restrict__ wu, char* __restrict__ ws, int rt, int cg, lptr sm, u64 tag) {
  constexpr int BM = ISQ ? 256 : 160;
  constexpr int MT = ISQ ? 4 : 5, NT = ISQ ? 5 : 4;
  constexpr int NWN = ISQ ? 2 : 4;
  constexpr int ABYTES = BM * 128;
  const int tid = threadIdx.x, lane = tid & 63;
  const int wid = __builtin_amdgcn_readfirstlane(tid >> 6);
  const int wm = wid / NWN, wn = wid % NWN;
  const int row0 = rt * BM;
  {
    StatLoads SL;
    p0_issue(x, wqkv, wg, wu, SL);
    p0_compute(SL, ws, tag, sm + P1_STATS);
  }
  const int r = lane & 15, g = lane >> 4, q = (lane & 15) >> 2, pp = lane & 3;
  unsigned xa0 = (unsigned)(size_t)sm + (unsigned)((wm * MT * 16 + r) * 128 + ((g ^ (r >> 1)) * 16));
  unsigned xa1 = xa0 ^ 64u;
  unsigned wa[2];
  if (ISQ) {
#pragma unroll
    for (int j = 0; j < 2; ++j) {
      int nt = 5 * wn + j;
      wa[j] = (unsigned)(size_t)sm + (unsigned)(ABYTES + (8 * g + q) * 320 + ((nt ^ (g & 1)) * 32) + pp * 8);
    }
  } else {
#pragma unroll
    for (int j = 0; j < 2; ++j) {
      int u = wn >> 1;
      int nt = 2 * (wn & 1) + j;
      int f2 = (q >> 1) | ((g & 1) << 1);
      wa[j] = (unsigned)(size_t)sm + (unsigned)(ABYTES + (u * 8 + g) * 1024 + q * 128 + ((nt ^ f2) * 32) + pp * 8);
    }
  }
  constexpr int W_T = ISQ ? 4 * 320 : 512;
  constexpr int W_S = ISQ ? 32 * 320 : 4096;
  constexpr int W_J = ISQ ? 64 : 16384;

  f32x4 acc[MT][NT];
#pragma unroll
  for (int i = 0; i < MT; ++i)
#pragma unroll
    for (int j = 0; j < NT; ++j) acc[i][j] = f32x4{0.f, 0.f, 0.f, 0.f};

  u32x4 xA[MT], xB[MT];
  u32x2 wAl[NT], wAh[NT], wBl[NT], wBh[NT];
  constexpr int NRD = 2 * NT + MT;
  auto rd = [&](int ri, u32x4* xn, u32x2* wl, u32x2* wh, unsigned xad, int s) {
    if (ri == 2) { xn[0] = lds_u128(lp(xad)); return; }
    int k = ri < 2 ? ri : ri - 1;
    if (k < 2 * NT) {
      int j = k >> 1;
      u32x2 v = lds_tru(lp(wa[j & 1]) + (j >> 1) * W_J + s * W_S + (k & 1) * W_T);
      if (k & 1) wh[j] = v; else wl[j] = v;
    } else {
      int i = k - 2 * NT + 1;
      xn[i] = lds_u128(lp(xad) + i * 2048);
    }
  };
  auto kstep = [&](const u32x4* xc, const u32x2* wcl, const u32x2* wch, u32x4* xn, u32x2* wnl, u32x2* wnh,
                   unsigned xad, int sn) {
#pragma unroll
    for (int k = 0; k < 20; ++k) {
      const int i = k / NT, j = k % NT;
      acc[i][j] = __builtin_amdgcn_mfma_f32_16x16x32_bf16(cat_u(wcl[j], wch[j]), __builtin_bit_cast(bf16x8, xc[i]), acc[i][j], 0, 0, 0);
      __builtin_amdgcn_sched_barrier(0);
      if (k < NRD) rd(k, xn, wnl, wnh, xad, sn);
      __builtin_amdgcn_sched_barrier(0);
    }
  };

  BARRIER();
  p0_publish(ws, tag, sm + P1_STATS);
#pragma unroll
  for (int ri = 0; ri < NRD; ++ri) rd(ri, xA, wAl, wAh, xa0, 0);
  int so_cur = 0;
#pragma unroll 1
  for (int t = 0; t < 10; ++t) {
    const int so_n = (so_cur == 2 * P1_STAGE) ? 0 : so_cur + P1_STAGE;
    const int dlt = so_n - so_cur;
    kstep(xA, wAl, wAh, xB, wBl, wBh, xa1, 1);
    xa0 += dlt; xa1 += dlt; wa[0] += dlt; wa[1] += dlt;
    BARRIER();
    kstep(xB, wBl, wBh, xA, wAl, wAh, xa0, 0);
    so_cur = so_n;
  }

  LAS f32x2* rowstat = (LAS f32x2*)(sm + P1_STATS);
  LAS float* colsum = (LAS float*)(sm + P1_STATS + 2048);
  f32x4 csv[NT];
#pragma unroll
  for (int j = 0; j < NT; ++j) {
    int nb = ISQ ? 5 * wn + j : (j >> 1) * 8 + 2 * wn + (j & 1);
    csv[j] = *(LAS f32x4*)(colsum + nb * 16 + 4 * g);
  }
  if (ISQ) {
    const int which = cg >> 2;
    const bool odd = lane & 1;
#pragma unroll
    for (int i = 0; i < MT; i += 2) {
      const int ml = wm * MT * 16 + i * 16 + r;
      const f32x2 sa = rowstat[ml], sb = rowstat[ml + 16];
      const float qs = which == 0 ? (0.22360679774997896f * 1.4426950408889634f) : 1.0f;
      const float ra = sa[1] * qs, rb = sb[1] * qs;
      const int m = row0 + ml + (odd ? 15 : 0);
#pragma unroll
      for (int j = 0; j < NT; ++j) {
        int col = (5 * wn + j) * 16 + 4 * g;
        int hl = col / 20, quad = (col - hl * 20) >> 2;
        int head = (cg & 3) * 8 + hl;
        float ya[4], yb[4];
#pragma unroll
        for (int e = 0; e < 4; ++e) {
          ya[e] = ra * (acc[i][j][e] - sa[0] * csv[j][e]);
          yb[e] = rb * (acc[i + 1][j][e] - sb[0] * csv[j][e]);
        }
        unsigned a0 = pack2(ya[0], ya[1]), a1 = pack2(ya[2], ya[3]);
        unsigned b0 = pack2(yb[0], yb[1]), b1 = pack2(yb[2], yb[3]);
        unsigned g0 = odd ? a0 : b0, g1 = odd ? a1 : b1;
        unsigned r0 = (unsigned)__builtin_amdgcn_update_dpp(0, (int)g0, 0xB1, 0xf, 0xf, false);
        unsigned r1 = (unsigned)__builtin_amdgcn_update_dpp(0, (int)g1, 0xB1, 0xf, 0xf, false);
        u32x4 o4 = odd ? u32x4{r0, r1, b0, b1} : u32x4{a0, a1, r0, r1};
        st16_dev(ws + WS_QKV + (size_t)((which * 32 + head) * 5 + quad) * QKV_PLANE + (size_t)m * 8, o4);
      }
    }
  } else {
#pragma unroll
    for (int i = 0; i < MT; ++i) {
      const int ml = wm * MT * 16 + i * 16 + r;
      const f32x2 st = rowstat[ml];
      const int m = row0 + ml;
      unsigned pk[2][2];
#pragma unroll
      for (int tp = 0; tp < 2; ++tp) {
        float hv[4];
#pragma unroll
        for (int e = 0; e < 4; ++e) {
          float gt = st[1] * (acc[i][tp][e] - st[0] * csv[tp][e]);
          float up = st[1] * (acc[i][tp + 2][e] - st[0] * csv[tp + 2][e]);
          float sg = __builtin_amdgcn_rcpf(1.0f + __expf(-gt));
          hv[e] = gt * sg * up;
        }
        pk[tp][0] = pack2(hv[0], hv[1]); pk[tp][1] = pack2(hv[2], hv[3]);
      }
      auto s0 = __builtin_amdgcn_permlane16_swap(pk[0][0], pk[1][0], false, false);
      auto s1 = __builtin_amdgcn_permlane16_swap(pk[0][1], pk[1][1], false, false);
      u32x4 o4 = {s0[0], s1[0], s0[1], s1[1]};
      if (m < SEQ) {
        int ks = 20 + 4 * cg + wn;
        int gg = 2 * (g & 1) + (g >> 1);
        st16_dev(ws + WS_A2 + ((size_t)(m >> 4) * KS2 + ks) * 1024 + (gg * 16 + r) * 16, o4);
      }
    }
  }
  if (tid == 0) { *(volatile LAS unsigned*)(sm + P2_GO) = 0u; *(volatile LAS unsigned*)(sm + P2_GO + 4) = 0u; }
  WAITV(0);
  BARRIER();
  if (tid == 0)
    __hip_atomic_store((u64*)(ws + (ISQ ? WS_FLAGQ : WS_FLAGG) + rt * 128 + cg * 8), tag, __ATOMIC_RELAXED, __HIP_MEMORY_SCOPE_AGENT);
}

template <typename F>
DI void p2_compute(char* __restrict__ ws, int pi, int hp, lptr sm, u64 tag, F&& before_store) {
  const int tid = threadIdx.x, lane = tid & 63;
  const int wid = __builtin_amdgcn_readfirstlane(tid >> 6);
  if (tid < 384) *(LAS u32x2*)(sm + 20 * P2_PSTRIDE + tid * 8) = u32x2{0x00003f80u, 0u};
  for (unsigned spins = 0; spins < (SPIN_MAX << 6); ++spins) {
    if (*(volatile LAS unsigned*)(sm + P2_GO) != 0u) break;
    __builtin_amdgcn_s_sleep(2);
  }
  asm volatile("" ::: "memory");
  p2_stage(ws, pi, hp, sm, wid);
  BARRIER();
  const int qt = wid & 3, hh = wid >> 2;
  const int head = hp * 2 + hh;
  const int q0 = pi * 128 + qt * 32;
  const int c = lane & 31, h = lane >> 5;
  bf16x8 qf0; s16x4 qf1;
  {
    lptr qb = sm + P2_QOFF + hh * 5 * 1024 + (qt * 32 + c) * 8;
    u32x2 a = *(LAS u32x2*)(qb + (2 * h) * 1024);
    u32x2 b = *(LAS u32x2*)(qb + (2 * h + 1) * 1024);
    u32x2 d = *(LAS u32x2*)(qb + 4 * 1024);
    u32x4 v0 = {a[0], a[1], b[0], b[1]};
    u32x2 v1 = {h ? 0u : d[0], h ? 0u : d[1]};
    qf0 = __builtin_bit_cast(bf16x8, v0);
    qf1 = __builtin_bit_cast(s16x4, v1);
  }
  const int kt_min = q0 >= 256 ? 0 : (256 - q0) >> 5;
  lptr kb = sm + (hh * 10) * P2_PSTRIDE + (qt * 32 + c) * 8;
  lptr vb;
  {
    const int blk = (lane >> 4) & 1, q = (lane & 15) >> 2, pp = lane & 3;
    int pl = blk ? (pp == 0 ? hh * 10 + 9 : 20) : hh * 10 + 5 + pp;
    vb = sm + pl * P2_PSTRIDE + (qt * 32 + 4 * h + q) * 8;
  }
  u32x4 kf0[9]; u32x2 kf1[9];
  auto load_k = [&]() {
#pragma unroll
    for (int kt = 0; kt < 9; ++kt) {
      u32x2 a = *(LAS u32x2*)(kb + (2 * h) * P2_PSTRIDE + kt * 256);
      u32x2 b = *(LAS u32x2*)(kb + (2 * h + 1) * P2_PSTRIDE + kt * 256);
      kf1[kt] = *(LAS u32x2*)(kb + 4 * P2_PSTRIDE + kt * 256);
      kf0[kt] = u32x4{a[0], a[1], b[0], b[1]};
    }
  };
  load_k();
  auto scores = [&](int kt, const f32x16& cin) {
    f32x16 z = __builtin_amdgcn_mfma_f32_32x32x16_bf16(__builtin_bit_cast(bf16x8, kf0[kt]), qf0, cin, 0, 0, 0);
    return __builtin_amdgcn_mfma_f32_32x32x8bf16_1k(__builtin_bit_cast(s16x4, kf1[kt]), qf1, z, 0, 0, 0);
  };
  f32x16 zero16;
#pragma unroll
  for (int e = 0; e < 16; ++e) zero16[e] = 0.f;
  f32x16 O = zero16;
  auto pv = [&](int kt, const u32x4& p0, const u32x4& p1) {
    u32x2 lo = lds_tru(vb + kt * 256), hi = lds_tru(vb + kt * 256 + 64);
    u32x2 lo2 = lds_tru(vb + kt * 256 + 128), hi2 = lds_tru(vb + kt * 256 + 192);
    O = __builtin_amdgcn_mfma_f32_32x32x16_bf16(cat_u(lo, hi), __builtin_bit_cast(bf16x8, p0), O, 0, 0, 0);
    O = __builtin_amdgcn_mfma_f32_32x32x16_bf16(cat_u(lo2, hi2), __builtin_bit_cast(bf16x8, p1), O, 0, 0, 0);
  };
#define SB() __builtin_amdgcn_sched_barrier(0)
  auto scoresc_asm = [&](int kt, const f32x16& cin) {
    f32x16 z;
    asm volatile("s_nop 1\n\tv_mfma_f32_32x32x16_bf16 %0, %1, %2, %5\n\tv_mfma_f32_32x32x8_bf16 %0, %3, %4, %0"
                 : "=&v"(z) : "v"(kf0[kt]), "v"(qf0), "v"(kf1[kt]), "v"(qf1), "v"(cin) : "memory");
    return z;
  };
#define MFMA_PAD() asm volatile("s_nop 15\n\ts_nop 3" ::: "memory")
  auto attend_full = [&]() {
#pragma unroll
    for (int kt = 0; kt < 9; ++kt) asm volatile("" : "+v"(kf0[kt]), "+v"(kf1[kt]));
    asm volatile("s_nop 1" : "+v"(qf0), "+v"(qf1));
    float mx;
    {
      float kn = 0.f;
#pragma unroll
      for (int kt = 0; kt < 9; ++kt) {
        float a = dot2(kf0[kt][0], kf0[kt][0], 0.f), b = dot2(kf0[kt][1], kf0[kt][1], 0.f);
        a = dot2(kf0[kt][2], kf0[kt][2], a); b = dot2(kf0[kt][3], kf0[kt][3], b);
        a = dot2(kf1[kt][0], kf1[kt][0], a); b = dot2(kf1[kt][1], kf1[kt][1], b);
        kn = fmaxf(kn, a + b);
      }
      kn = max_ror<1>(kn); kn = max_ror<2>(kn); kn = max_ror<4>(kn); kn = max_ror<8>(kn); kn = max_x16(kn);
      kn = add_x32(kn);
      const u32x4 qa = __builtin_bit_cast(u32x4, qf0); const u32x2 qb = __builtin_bit_cast(u32x2, qf1);
      float qn = dot2(qa[0], qa[0], 0.f);
      qn = dot2(qa[1], qa[1], qn); qn = dot2(qa[2], qa[2], qn); qn = dot2(qa[3], qa[3], qn);
      qn = dot2(qb[0], qb[0], qn); qn = dot2(qb[1], qb[1], qn);
      qn = add_x32(qn);
      mx = __builtin_amdgcn_sqrtf(qn * kn) * 1.001f;
    }
    f32x16 nm16;
#pragma unroll
    for (int e = 0; e < 16; ++e) nm16[e] = -mx;
    struct VF { u32x2 a, b, c, d; };
    auto vread = [&](int kt) {
      VF v;
      v.a = lds_tru(vb + kt * 256); v.b = lds_tru(vb + kt * 256 + 64);
      v.c = lds_tru(vb + kt * 256 + 128); v.d = lds_tru(vb + kt * 256 + 192);
      return v;
    };
    auto pvv = [&](const VF& v, const u32x4& p0, const u32x4& p1) {
      O = __builtin_amdgcn_mfma_f32_32x32x16_bf16(cat_u(v.a, v.b), __builtin_bit_cast(bf16x8, p0), O, 0, 0, 0);
      O = __builtin_amdgcn_mfma_f32_32x32x16_bf16(cat_u(v.c, v.d), __builtin_bit_cast(bf16x8, p1), O, 0, 0, 0);
    };
    VF vn = vread(1), v0;
    SB();
    f32x16 sn = scoresc_asm(1, nm16);
    f32x16 s0;
#pragma unroll
    for (int kt = 1; kt < 8; ++kt) {
      f32x16 sv = sn;
      VF vc = vn;
      SB();
      vn = vread(kt < 7 ? kt + 1 : 8);
      if (kt == 7) v0 = vread(0);
      sn = scoresc_asm(kt < 7 ? kt + 1 : 8, nm16);
      if (kt == 7) s0 = scoresc_asm(0, nm16);
      if (kt == 1) MFMA_PAD();
      SB();
      u32x4 pa, pb;
#pragma unroll
      for (int e = 0; e < 4; ++e) {
        pa[e] = pack2(__builtin_amdgcn_exp2f(sv[2 * e]), __builtin_amdgcn_exp2f(sv[2 * e + 1]));
        pb[e] = pack2(__builtin_amdgcn_exp2f(sv[8 + 2 * e]), __builtin_amdgcn_exp2f(sv[8 + 2 * e + 1]));
      }
      pvv(vc, pa, pb);
      if (kt == 7) { SB(); before_store(); }
    }
    SB();
    {
      f32x16 s8 = sn;
#pragma unroll
      for (int e = 0; e < 16; ++e) { int rr = (e & 3) + 8 * (e >> 2) + 4 * h; s8[e] = rr <= c ? s8[e] : s0[e]; }
      u32x4 pa, pb, qa, qb2;
#pragma unroll
      for (int e = 0; e < 8; ++e) {
        unsigned pk = pack2(__builtin_amdgcn_exp2f(s8[2 * e]), __builtin_amdgcn_exp2f(s8[2 * e + 1]));
        int r0 = ((2 * e) & 3) + 8 * ((2 * e) >> 2) + 4 * h;
        unsigned msk = (r0 <= c ? 0xffffu : 0u) | (r0 + 1 <= c ? 0xffff0000u : 0u);
        if (e < 4) { pa[e] = pk & msk; qa[e] = pk & ~msk; } else { pb[e - 4] = pk & msk; qb2[e - 4] = pk & ~msk; }
      }
      pvv(vn, pa, pb);
      pvv(v0, qa, qb2);
    }
  };
  auto attend = [&](auto FULLC) {
    constexpr bool FULL = decltype(FULLC)::value;
    const bool has0 = FULL || kt_min == 0;
    float mx;
    {
      f32x16 s8 = scores(8, zero16);
      if (has0) {
        f32x16 s0 = scores(0, zero16);
#pragma unroll
        for (int e = 0; e < 16; ++e) { int rr = (e & 3) + 8 * (e >> 2) + 4 * h; s8[e] = rr <= c ? s8[e] : s0[e]; }
      } else {
#pragma unroll
        for (int e = 0; e < 16; ++e) { int rr = (e & 3) + 8 * (e >> 2) + 4 * h; s8[e] = rr <= c ? s8[e] : -INFINITY; }
      }
      float m0 = fmaxf(s8[0], s8[1]), m1 = fmaxf(s8[2], s8[3]);
#pragma unroll
      for (int e = 4; e < 16; e += 4) { m0 = fmaxf(fmaxf(m0, s8[e]), s8[e + 1]); m1 = fmaxf(fmaxf(m1, s8[e + 2]), s8[e + 3]); }
#pragma unroll
      for (int kt = 1; kt < 8; ++kt) {
        if (FULL || kt >= kt_min) {
          f32x16 sv = scores(kt, zero16);
#pragma unroll
          for (int e = 0; e < 16; e += 4) { m0 = fmaxf(fmaxf(m0, sv[e]), sv[e + 1]); m1 = fmaxf(fmaxf(m1, sv[e + 2]), sv[e + 3]); }
        }
      }
      mx = max_x32(fmaxf(m0, m1));
    }
    f32x16 nm16;
#pragma unroll
    for (int e = 0; e < 16; ++e) nm16[e] = -mx;
    {
      f32x16 s8 = scores(8, nm16);
      if (has0) {
        f32x16 s0 = scores(0, nm16);
#pragma unroll
        for (int e = 0; e < 16; ++e) { int rr = (e & 3) + 8 * (e >> 2) + 4 * h; s8[e] = rr <= c ? s8[e] : s0[e]; }
      }
      u32x4 pa, pb, qa, qb2;
#pragma unroll
      for (int e = 0; e < 8; ++e) {
        unsigned pk = pack2(__builtin_amdgcn_exp2f(s8[2 * e]), __builtin_amdgcn_exp2f(s8[2 * e + 1]));
        int r0 = ((2 * e) & 3) + 8 * ((2 * e) >> 2) + 4 * h;
        unsigned msk = (r0 <= c ? 0xffffu : 0u) | (r0 + 1 <= c ? 0xffff0000u : 0u);
        if (e < 4) { pa[e] = pk & msk; qa[e] = pk & ~msk; } else { pb[e - 4] = pk & msk; qb2[e - 4] = pk & ~msk; }
      }
      pv(8, pa, pb);
      if (has0) pv(0, qa, qb2);
    }
#pragma unroll
    for (int kt = 1; kt < 8; ++kt) {
      if (FULL || kt >= kt_min) {
        f32x16 sv = scores(kt, nm16);
        u32x4 pa, pb;
#pragma unroll
        for (int e = 0; e < 4; ++e) {
          pa[e] = pack2(__builtin_amdgcn_exp2f(sv[2 * e]), __builtin_amdgcn_exp2f(sv[2 * e + 1]));
          pb[e] = pack2(__builtin_amdgcn_exp2f(sv[8 + 2 * e]), __builtin_amdgcn_exp2f(sv[8 + 2 * e + 1]));
        }
        pv(kt, pa, pb);
      }
    }
  };
  bool exact = kt_min != 0;
  if (!exact) {
    attend_full();
    const float ls = __shfl(O[8], c + 32);
    if (!__all(ls >= 0x1p-64f && ls <= 0x1p+16f)) { exact = true; O = zero16; load_k(); }
  }
  if (exact) { attend(std::false_type{}); before_store(); }
  float l = __shfl(O[8], c + 32);
  float inv = __builtin_amdgcn_rcpf(l);
  {
    const int m = q0 + c;
    char* ob = ws + WS_A2 + (size_t)(m >> 4) * KS2 * 1024 + (m & 15) * 16;
#pragma unroll
    for (int grp = 0; grp < 3; ++grp) {
      if (grp < 2 || h == 0) {
        int kidx = head * 20 + 8 * grp + 4 * h;
        u32x2 o2 = {pack2(O[4 * grp] * inv, O[4 * grp + 1] * inv), pack2(O[4 * grp + 2] * inv, O[4 * grp + 3] * inv)};
        st8_dev(ob + (kidx >> 5) * 1024 + ((kidx >> 3) & 3) * 256 + ((kidx >> 2) & 1) * 8, o2);
      }
    }
  }
  WAITV(0);
  BARRIER();
  if (tid == 0)
    __hip_atomic_store((u64*)(ws + WS_FLAGO + pi * 128 + hp * 8), tag, __ATOMIC_RELAXED, __HIP_MEMORY_SCOPE_AGENT);
}

constexpr int P3D = 5;
struct P3A { u32x4 f[P3D][3]; };
DI void p3_load_a(u32x4* dst, __amdgpu_buffer_rsrc_t rs, const unsigned* aoff, int st) {
  const int ks0 = st < 12 ? 20 + 4 * st : 4 * (st - 12);
#pragma unroll
  for (int i = 0; i < 3; ++i) dst[i] = __builtin_amdgcn_raw_buffer_load_b128(rs, aoff[i], ks0 * 1024, 16);
}
DI void p3_a_setup(unsigned* aoff, int rt) {
  const int lane = threadIdx.x & 63, wid = threadIdx.x >> 6;
#pragma unroll
  for (int i = 0; i < 3; ++i) {
    int gmt = rt * 6 + (wid >> 2) * 3 + i; gmt = gmt > 127 ? 127 : gmt;
    aoff[i] = (unsigned)((gmt * KS2 + (wid & 3)) * 1024 + lane * 16);
  }
}

DI void p3_compute(const u16* __restrict__ x, char* __restrict__ ws, u16* __restrict__ out, int rt, int ct, lptr sm,
                   P3A& A, __amdgpu_buffer_rsrc_t rs, const unsigned* aoff, u64 tag) {
  const int tid = threadIdx.x, lane = tid & 63;
  const int wid = __builtin_amdgcn_readfirstlane(tid >> 6);
  const int ks4 = wid & 3, mh = wid >> 2;
  const int r = lane & 15, g = lane >> 4, q = (lane & 15) >> 2, pp = lane & 3;
  unsigned wa[4];
#pragma unroll
  for (int j = 0; j < 4; ++j) {
    int f2 = (q >> 1) | ((g & 1) << 1);
    wa[j] = (unsigned)(size_t)sm + (unsigned)((ks4 * 4 + g) * 1024 + q * 128 + ((j ^ f2) * 32) + pp * 8);
  }
  f32x4 acc[3][4];
#pragma unroll
  for (int i = 0; i < 3; ++i)
#pragma unroll
    for (int j = 0; j < 4; ++j) acc[i][j] = f32x4{0.f, 0.f, 0.f, 0.f};
  u32x4 xres[2];
  const int r_lo = rt * 96, r_hi = (r_lo + 95 > SEQ - 1) ? SEQ - 1 : r_lo + 95;
  const char* oflp = ws + WS_FLAGO + (((lane >> 4) & 1) ? r_hi >> 7 : r_lo >> 7) * 128 + (lane & 15) * 8;
  u64 ofl = 0;
#pragma unroll
  for (int st = 3; st < P3D; ++st) p3_load_a(A.f[st], rs, aoff, st);
#pragma unroll
  for (int t = 0; t < 17; ++t) {
    if (t == 5 || t == 10 || t == 14) BARRIER();
    const int so = p3_slot(t) * P3_SLOT;
    u32x2 wl[4], wh[4];
#pragma unroll
    for (int j = 0; j < 4; ++j) {
      wl[j] = lds_tru(lp(wa[j] + so));
      wh[j] = lds_tru(lp(wa[j] + so) + 512);
    }
    u32x4 xc[3];
#pragma unroll
    for (int i = 0; i < 3; ++i) xc[i] = A.f[t % P3D][i];
    asm volatile("" : "+v"(xc[0]), "+v"(xc[1]), "+v"(xc[2]));
    if (t + P3D == 10) ofl = ld8_dev(oflp);
    if (t + P3D == 12) {
      if (!__all(ofl == tag)) wave_wait_flags(oflp, true, tag);
      asm volatile("" ::: "memory");
    }
    if (t + P3D < 17) p3_load_a(A.f[t % P3D], rs, aoff, t + P3D);
    if (t == 14) {
#pragma unroll
      for (int u = 0; u < 2; ++u) {
        int it = tid + 512 * u; it = it < 768 ? it : 0;
        int m = rt * 96 + (it >> 3); m = m > SEQ - 1 ? SEQ - 1 : m;
        xres[u] = *(const u32x4*)(x + (size_t)m * DM + ct * 64 + (it & 7) * 8);
      }
    }
#pragma unroll
    for (int i = 0; i < 3; ++i)
#pragma unroll
      for (int j = 0; j < 4; ++j)
        acc[i][j] = __builtin_amdgcn_mfma_f32_16x16x32_bf16(cat_u(wl[j], wh[j]), __builtin_bit_cast(bf16x8, xc[i]), acc[i][j], 0, 0, 0);
  }
  asm volatile("" : "+v"(xres[0]), "+v"(xres[1]));
  BARRIER();
  constexpr int RS = 68;
  {
    LAS float* red = (LAS float*)sm + ks4 * 96 * RS;
#pragma unroll
    for (int i = 0; i < 3; ++i)
#pragma unroll
      for (int j = 0; j < 4; ++j)
        *(LAS f32x4*)(red + ((mh * 3 + i) * 16 + r) * RS + j * 16 + 4 * g) = acc[i][j];
  }
  BARRIER();
#pragma unroll
  for (int u = 0; u < 2; ++u) {
    const int it = tid + 512 * u;
    int ml = it >> 3, c8 = it & 7;
    int m = rt * 96 + ml;
    if (it < 768 && m < SEQ) {
      LAS float* r0 = (LAS float*)sm + ml * RS + c8 * 8;
      f32x4 a0 = *(LAS f32x4*)r0, a1 = *(LAS f32x4*)(r0 + 4);
#pragma unroll
      for (int k = 1; k < 4; ++k) { a0 += *(LAS f32x4*)(r0 + k * 96 * RS); a1 += *(LAS f32x4*)(r0 + k * 96 * RS + 4); }
      const u32x4 xv = xres[u];
      u32x4 o;
#pragma unroll
      for (int e = 0; e < 4; ++e) {
        float lo = __uint_as_float(xv[e] << 16), hi = __uint_as_float(xv[e] & 0xffff0000u);
        float va = e < 2 ? a0[2 * e] : a1[2 * e - 4], vb2 = e < 2 ? a0[2 * e + 1] : a1[2 * e - 3];
        o[e] = pack2(lo + va, hi + vb2);
      }
      *(u32x4*)(out + (size_t)m * DM + ct * 64 + c8 * 8) = o;
    }
  }
}

__global__ __launch_bounds__(768, 3) void k_fused(const u16* __restrict__ x, const u16* __restrict__ wqkv,
                                                  const u16* __restrict__ wo, const u16* __restrict__ wg,
                                                  const u16* __restrict__ wu, const u16* __restrict__ wd,
                                                  char* __restrict__ ws, u16* __restrict__ out) {
  extern __shared__ __attribute__((aligned(16))) char smem[];
  lptr sm = (lptr)smem;
  u64 tag = hw_dispatch_id() * 0x9E3779B97F4A7C15ull + (u64)(size_t)__builtin_amdgcn_dispatch_ptr() * 0xC2B2AE3D27D4EB4Full;
  tag ^= tag >> 29;
  tag = (tag | 1ull) & ~0x100ull;
  const int wid = __builtin_amdgcn_readfirstlane(threadIdx.x >> 6);
  if (wid >= 8) {
    loader_main(x, wqkv, wo, wg, wu, wd, ws, sm, tag, wid - 8);
    return;
  }
  const Work W = get_work();
  if (W.p1type == 0) p1_compute<true>(x, wqkv, wg, wu, ws, W.p1rt, W.p1cg, sm, tag);
  else if (W.p1type == 1) p1_compute<false>(x, wqkv, wg, wu, ws, W.p1rt, W.p1cg, sm, tag);
  else {
    StatLoads SL;
    p0_issue(x, wqkv, wg, wu, SL);
    p0_compute(SL, ws, tag, sm + P1_STATS);
    BARRIER();
    p0_publish(ws, tag, sm + P1_STATS);
    if (threadIdx.x == 0) { *(volatile LAS unsigned*)(sm + P2_GO) = 0u; *(volatile LAS unsigned*)(sm + P2_GO + 4) = 0u; }
    BARRIER();
  }
  P3A A3;
  unsigned aoff[3];
  p3_a_setup(aoff, W.p3rt >= 0 ? W.p3rt : 0);
  const __amdgpu_buffer_rsrc_t rs = __builtin_amdgcn_make_buffer_rsrc(ws + WS_A2, 0, 128 * KS2 * 1024, 0x00020000);
  p2_compute(ws, W.pi, W.hp, sm, tag, [&]() {
    if (W.p3rt >= 0) {
      for (unsigned spins = 0; spins < (SPIN_MAX << 6); ++spins) {
        if (*(volatile LAS unsigned*)(sm + P3_HGO) != 0u) break;
        __builtin_amdgcn_s_sleep(2);
      }
      asm volatile("" ::: "memory");
#pragma unroll
      for (int st = 0; st < 3; ++st) p3_load_a(A3.f[st], rs, aoff, st);
      asm volatile("" ::: "memory");
      return true;
    }
    return false;
  });
  if (W.p3rt >= 0) p3_compute(x, ws, out, W.p3rt, W.p3ct, sm, A3, rs, aoff, tag);
}

extern "C" void kernel_launch(void* const* d_in, const int* in_sizes, int n_in,
                              void* d_out, int out_size, void* d_ws, size_t ws_size,
                              hipStream_t stream) {
  const u16* x = (const u16*)d_in[0];
  const u16* wqkv = (const u16*)d_in[1];
  const u16* wo = (const u16*)d_in[2];
  const u16* wg = (const u16*)d_in[4];
  const u16* wu = (const u16*)d_in[5];
  const u16* wd = (const u16*)d_in[6];
  (void)hipFuncSetAttribute((const void*)k_fused, hipFuncAttributeMaxDynamicSharedMemorySize, LDS_TOTAL);
  k_fused<<<256, 768, LDS_TOTAL, stream>>>(x, wqkv, wo, wg, wu, wd, (char*)d_ws, (u16*)d_out);
}
```
